```python
import torch

def _softmax(x, axis=-1):
    return torch.softmax(x, dim=axis)
def _gelu(x):
    return torch.nn.functional.gelu(x, approximate="tanh")
def _silu(x):
    return torch.nn.functional.silu(x)
def _sigmoid(x):
    return torch.sigmoid(x)
def _layernorm(x, axis=-1, eps=1e-5):
    m = x.mean(dim=axis, keepdim=True)
    v = x.var(dim=axis, keepdim=True, unbiased=False)
    return (x - m) / torch.sqrt(v + eps)
def _scatter_add(tgt, idx, src, axis=0):
    return tgt.clone().index_add_(axis, idx, src)
def _segment_sum(src, seg, n):
    out = torch.zeros((n,) + src.shape[1:], dtype=src.dtype)
    return out.index_add_(0, seg, src)
def _pad(x, pw):
    flat = tuple(v for pair in reversed(pw) for v in pair)
    return torch.nn.functional.pad(x, flat)

kernel_name = 'standalone__cheap_equivariant_egnn__graph'

def setup_inputs():

    _w = (lambda a: (lambda t: t.to(torch.bfloat16) if t.is_floating_point() else t)(torch.as_tensor(a)))
    N, deg, D, De = 25000, 32, 64, max(4, 1)
    E = N * deg
    return {
        "h": _w((lambda *s: torch.randn(*s, dtype=torch.bfloat16) * 0.2)(N, D)),
        "pos": _w((lambda *s: torch.randn(*s, dtype=torch.bfloat16) * 0.8)(N, 3)),
        "e_attr": _w((lambda *s: torch.randn(*s, dtype=torch.bfloat16) * 0.8)(E, De)),
        "src": _w((lambda lo, hi, *s: torch.randint(lo, hi, s))(0, N, E).to(torch.int64)),
        "dst": _w((lambda lo, hi, *s: torch.randint(lo, hi, s))(0, N, E).to(torch.int64)),
        "w_e": _w(0.1 * (lambda *s: torch.randn(*s, dtype=torch.bfloat16) * 0.8)(2 * D + 1 + De, D)),
        "w_x": _w(0.1 * (lambda *s: torch.randn(*s, dtype=torch.bfloat16) * 0.2)(D, 1)),
        "w_h": _w(0.1 * (lambda *s: torch.randn(*s, dtype=torch.bfloat16) * 0.2)(2 * D, D)),
    }

def reference(h, pos, e_attr, src, dst, w_e, w_x, w_h):
    N, D = h.shape[0], h.shape[1]
    diff = pos[dst] - pos[src]
    dist2 = torch.sum(diff * diff, axis=-1, keepdims=True)
    e_in = torch.concatenate([h[dst], h[src], dist2, e_attr], axis=-1)
    m_ij = _silu(e_in @ w_e)
    coord_scale = m_ij @ w_x
    pos_new = pos + _segment_sum(diff * coord_scale, dst, N)
    m_i = _segment_sum(m_ij, dst, N)
    h_new = h + _silu(torch.concatenate([h, m_i], axis=-1) @ w_h)
    return h_new, pos_new
```

```cpp
#include <hip/hip_runtime.h>
#include <hip/hip_bf16.h>
#include <stdint.h>

typedef unsigned short u16;
__device__ __forceinline__ float bf2f(u16 x){ return __uint_as_float(((unsigned)x)<<16); }
__device__ __forceinline__ u16 f2bf(float x){ unsigned u=__float_as_uint(x); u += 0x7fffu + ((u>>16)&1u); return (u16)(u>>16); }
__device__ __forceinline__ float rbf(float x){ return bf2f(f2bf(x)); }
__device__ __forceinline__ float silu(float x){ return x/(1.f+__expf(-x)); }

__global__ void edge_k(const u16* h,const u16* pos,const u16* ea,const int* src,const int* dst,const u16* we,const u16* wx,
                       float* macc,float* pacc,int E){
  int wave = (blockIdx.x*blockDim.x+threadIdx.x)>>6; int lane=threadIdx.x&63;
  int nw = (gridDim.x*blockDim.x)>>6;
  float wxl = bf2f(wx[lane]);
  for(int e=wave;e<E;e+=nw){
    int s=src[e], d=dst[e];
    float dx=rbf(bf2f(pos[d*3+0])-bf2f(pos[s*3+0]));
    float dy=rbf(bf2f(pos[d*3+1])-bf2f(pos[s*3+1]));
    float dz=rbf(bf2f(pos[d*3+2])-bf2f(pos[s*3+2]));
    float d2=rbf(rbf(dx*dx)+rbf(dy*dy)+rbf(dz*dz));
    float acc=0.f;
    for(int k=0;k<64;k++) acc += bf2f(h[d*64+k])*bf2f(we[k*64+lane]);
    for(int k=0;k<64;k++) acc += bf2f(h[s*64+k])*bf2f(we[(64+k)*64+lane]);
    acc += d2*bf2f(we[128*64+lane]);
    for(int k=0;k<4;k++) acc += bf2f(ea[e*4+k])*bf2f(we[(129+k)*64+lane]);
    float m = rbf(silu(rbf(acc)));
    float c = m*wxl;
    for(int o=32;o>0;o>>=1) c += __shfl_xor(c,o);
    c = rbf(c);
    atomicAdd(&macc[d*64+lane], m);
    if(lane<3){ float df = lane==0?dx:(lane==1?dy:dz); atomicAdd(&pacc[d*3+lane], rbf(df*c)); }
  }
}
__global__ void node_k(const u16* h,const u16* pos,const u16* wh,const float* macc,const float* pacc,u16* out,int N){
  int wave = (blockIdx.x*blockDim.x+threadIdx.x)>>6; int lane=threadIdx.x&63;
  if(wave>=N) return;
  int n=wave;
  float acc=0.f;
  for(int k=0;k<64;k++) acc += bf2f(h[n*64+k])*bf2f(wh[k*64+lane]);
  for(int k=0;k<64;k++) acc += rbf(macc[n*64+k])*bf2f(wh[(64+k)*64+lane]);
  float u = rbf(silu(rbf(acc)));
  out[n*64+lane]=f2bf(bf2f(h[n*64+lane])+u);
  if(lane<3) out[N*64+n*3+lane]=f2bf(bf2f(pos[n*3+lane])+rbf(pacc[n*3+lane]));
}
extern "C" void kernel_launch(void* const* d_in, const int* in_sizes, int n_in,
                              void* d_out, int out_size, void* d_ws, size_t ws_size,
                              hipStream_t stream) {
  const u16* h=(const u16*)d_in[0]; const u16* pos=(const u16*)d_in[1]; const u16* ea=(const u16*)d_in[2];
  const int* src=(const int*)d_in[3]; const int* dst=(const int*)d_in[4];
  const u16* we=(const u16*)d_in[5]; const u16* wx=(const u16*)d_in[6]; const u16* wh=(const u16*)d_in[7];
  int N=in_sizes[0]/64, E=in_sizes[3];
  float* macc=(float*)d_ws; float* pacc=macc+(size_t)N*64;
  hipMemsetAsync(d_ws,0,(size_t)N*67*4,stream);
  edge_k<<<2048,256,0,stream>>>(h,pos,ea,src,dst,we,wx,macc,pacc,E);
  node_k<<<(N*64+255)/256,256,0,stream>>>(h,pos,wh,macc,pacc,(u16*)d_out,N);
}
```

```cpp
#include <hip/hip_runtime.h>
#include <stdint.h>

typedef unsigned short u16;
typedef unsigned char u8;
typedef unsigned int u32;
typedef short s16x4 __attribute__((ext_vector_type(4)));
typedef __bf16 bf16x2 __attribute__((ext_vector_type(2)));
typedef __bf16 bf16x8 __attribute__((ext_vector_type(8)));
typedef float f32x2 __attribute__((ext_vector_type(2)));
typedef float f32x4 __attribute__((ext_vector_type(4)));
typedef float f32x16 __attribute__((ext_vector_type(16)));
typedef u32 u32x2 __attribute__((ext_vector_type(2)));
typedef u32 u32x4 __attribute__((ext_vector_type(4)));
typedef int i32x4 __attribute__((ext_vector_type(4)));
#define LAS __attribute__((address_space(3)))
#define DI __device__ __forceinline__

constexpr int NB    = 98;
constexpr int NBUCK = 256;
constexpr int CH    = 3128;
constexpr int NCH   = 256;
constexpr int CAP   = 3584;
constexpr int MAXR  = 12;
constexpr float NLOG2E = -1.4426950408889634f;
constexpr float NLN2   = -0.6931471805599453f;

DI float bf2f(u32 bits16) { return __uint_as_float(bits16 << 16); }
DI u32 pk_bf16(float lo, float hi) {
  f32x2 v; v[0] = lo; v[1] = hi;
  return __builtin_bit_cast(u32, __builtin_convertvector(v, bf16x2));
}
DI int dpp_add(int x, int v) { return x + v; }
template <int CTRL, int RMASK> DI int dpp_i(int x) { return __builtin_amdgcn_update_dpp(0, x, CTRL, RMASK, 0xf, false); }
template <int CTRL, int RMASK> DI float dpp_f(float x) {
  return __builtin_bit_cast(float, __builtin_amdgcn_update_dpp(0, __builtin_bit_cast(int, x), CTRL, RMASK, 0xf, false));
}
DI int wave_incl_scan(int x) {
  x += dpp_i<0x111, 0xf>(x); x += dpp_i<0x112, 0xf>(x); x += dpp_i<0x114, 0xf>(x); x += dpp_i<0x118, 0xf>(x);
  x += dpp_i<0x142, 0xa>(x); x += dpp_i<0x143, 0xc>(x);
  return x;
}
DI float half_reduce(float x) {
  x += dpp_f<0x111, 0xf>(x); x += dpp_f<0x112, 0xf>(x); x += dpp_f<0x114, 0xf>(x); x += dpp_f<0x118, 0xf>(x);
  x += dpp_f<0x142, 0xa>(x);
  return x;
}
DI void glds16(u32 lds_addr, u32 voff, const void* base) {
  asm volatile("s_mov_b32 m0, %0\n\ts_nop 0\n\tglobal_load_lds_dwordx4 %1, %2" ::"s"(lds_addr), "v"(voff), "s"(base) : "memory");
}
DI s16x4 tr_read(const char* p) { return __builtin_amdgcn_ds_read_tr16_b64_v4i16((LAS s16x4*)p); }
DI bf16x8 mk8(s16x4 lo, s16x4 hi) {
  return __builtin_bit_cast(bf16x8, __builtin_shufflevector(lo, hi, 0, 1, 2, 3, 4, 5, 6, 7));
}
#define MFMA(a, b, c) __builtin_amdgcn_mfma_f32_32x32x16_bf16((a), (b), (c), 0, 0, 0)

constexpr int K1_TAB = 147 * 1024;
constexpr int K1_HIST = K1_TAB;
constexpr int K1_OFF = K1_HIST + 1024;
constexpr int K1_LDS = K1_OFF + 1024;
constexpr int K1_GST = CH * 16;

__global__ __launch_bounds__(1024) void k1_partition(const u16* __restrict__ pos, const u32x4* __restrict__ ea4,
                                                     const i32x4* __restrict__ src4, const i32x4* __restrict__ dst4,
                                                     u32x4* __restrict__ R, u8* __restrict__ G, u32* __restrict__ T,
                                                     int N, int E) {
  extern __shared__ __attribute__((aligned(16))) char lds[];
  const int tid = threadIdx.x, lane = tid & 63;
  const int wv = __builtin_amdgcn_readfirstlane(tid >> 6);
  const int w = blockIdx.x;
  u32* hist = (u32*)(lds + K1_HIST);
  u32* off = (u32*)(lds + K1_OFF);
  const u32 lds0 = (u32)(uintptr_t)(LAS char*)lds;

  const int e0 = w * CH + 4 * tid;
  const bool act = (tid < CH / 4) && (e0 < E);
  i32x4 d4 = {0, 0, 0, 0}, s4 = {0, 0, 0, 0};
  u32x4 a0 = {0, 0, 0, 0}, a1 = {0, 0, 0, 0};
  if (act) {
    d4 = dst4[e0 >> 2];
    s4 = src4[e0 >> 2];
    a0 = ea4[e0 >> 1];
    a1 = ea4[(e0 >> 1) + 1];
  }
  {
    const int tb = N * 6;
    const int np = (tb + 1023) >> 10;
    for (int p = wv; p < np; p += 16) {
      u32 o = p * 1024 + lane * 16;
      if (o > (u32)(tb - 16)) o = tb - 16;
      glds16(lds0 + p * 1024, o, pos);
    }
  }
  if (tid < 256) hist[tid] = 0;
  __syncthreads();

  int bk[4], gl[4], rk[4];
#pragma unroll
  for (int i = 0; i < 4; i++) {
    const u32 d = (u32)d4[i];
    bk[i] = (int)((d * 42800u) >> 22);
    gl[i] = (int)d - bk[i] * NB;
    rk[i] = 0;
  }
  if (act) {
#pragma unroll
    for (int i = 0; i < 4; i++) rk[i] = atomicAdd(&hist[bk[i]], 1u);
  }
  asm volatile("s_waitcnt vmcnt(0)" ::: "memory");
  __syncthreads();

  {
    u32x4 c = *(const u32x4*)(hist + 4 * lane);
    int s = c[0] + c[1] + c[2] + c[3];
    int inc = wave_incl_scan(s);
    u32x4 o;
    o[0] = inc - s; o[1] = o[0] + c[0]; o[2] = o[1] + c[1]; o[3] = o[2] + c[2];
    *(u32x4*)(off + 4 * lane) = o;
    if (wv == 0) {
#pragma unroll
      for (int i = 0; i < 4; i++) T[(4 * lane + i) * NCH + w] = o[i] | (c[i] << 16);
    }
  }
  u32x4 rec[4];
  int slot[4];
#pragma unroll
  for (int i = 0; i < 4; i++) {
    const u32 s = (u32)s4[i];
    const u32 a = (s * 6u) & ~3u;
    const u32 d0 = *(const u32*)(lds + a), d1 = *(const u32*)(lds + a + 4);
    const u32 sh = (s & 1u) * 16u;
    const u32 xy = __builtin_amdgcn_alignbit(d1, d0, sh);
    const u32 z = (d1 >> sh) & 0xffffu;
    rec[i][0] = i < 2 ? a0[2 * i] : a1[2 * i - 4];
    rec[i][1] = i < 2 ? a0[2 * i + 1] : a1[2 * i - 3];
    rec[i][2] = xy;
    rec[i][3] = z | (s << 16);
    slot[i] = off[bk[i]] + rk[i];
  }
  __syncthreads();
  if (act) {
#pragma unroll
    for (int i = 0; i < 4; i++) {
      *(u32x4*)(lds + slot[i] * 16) = rec[i];
      *(u8*)(lds + K1_GST + slot[i]) = (u8)gl[i];
    }
  }
  __syncthreads();
  int cnt = E - w * CH; cnt = cnt > CH ? CH : cnt; cnt = cnt < 0 ? 0 : cnt;
  u32x4* Rw = R + (size_t)w * CH;
  for (int i = tid; i < cnt; i += 1024) Rw[i] = *(const u32x4*)(lds + i * 16);
  if (tid < (cnt + 7) / 8) *(u32x2*)(G + (size_t)w * CH + tid * 8) = *(const u32x2*)(lds + K1_GST + tid * 8);
}

constexpr int L_GB = 0;
constexpr int L_V = L_GB + 8 * 4096;
constexpr int L_EA = L_V + CAP * 8;
constexpr int L_DG = L_EA + CAP * 8;
constexpr int L_SRC = L_DG + CAP * 4;
constexpr int L_AP = L_SRC + (CAP + 32) * 2;
constexpr int L_MACC = L_AP + 106 * 128;
constexpr int MS = 68;
constexpr int L_PACC = L_MACC + 106 * MS * 4;
constexpr int L_HIST = L_PACC + 106 * 16;
constexpr int L_OFF = L_HIST + 144 * 4;
constexpr int L_ZERO = L_OFF + 144 * 4;
constexpr int L_PD = L_ZERO + 512;
constexpr int L_MISC = L_PD + 128 * 8;
constexpr int K2_LDS = L_MISC + 256;
static_assert(K2_LDS <= 163840, "LDS");
static_assert(L_AP % 16 == 0 && L_MACC % 16 == 0 && L_V % 16 == 0 && L_SRC % 16 == 0, "align");
constexpr int L_WE = L_V;
constexpr int L_HT = L_V + 136 * 128;
constexpr int L_DESC = L_GB;

DI float silu_scaled_pair_to(f32x2 z, u32& out) { return 0; }

DI u32 silu2(float z0, float z1) {
  f32x2 z; z[0] = z0; z[1] = z1;
  f32x2 t; t[0] = __builtin_amdgcn_exp2f(z0); t[1] = __builtin_amdgcn_exp2f(z1);
  t = t + 1.0f;
  f32x2 r; r[0] = __builtin_amdgcn_rcpf(t[0]); r[1] = __builtin_amdgcn_rcpf(t[1]);
  f32x2 m = z * r;
  return __builtin_bit_cast(u32, __builtin_convertvector(m, bf16x2));
}

__global__ __launch_bounds__(512) void k2_main(const u16* __restrict__ h, const u16* __restrict__ pos,
                                               const u16* __restrict__ we, const u16* __restrict__ wx,
                                               const u16* __restrict__ wh, const u32x4* __restrict__ R,
                                               const u8* __restrict__ G, const u32* __restrict__ T,
                                               u16* __restrict__ out, int N) {
  extern __shared__ __attribute__((aligned(16))) char lds[];
  const int tid = threadIdx.x, lane = tid & 63;
  const int wv = __builtin_amdgcn_readfirstlane(tid >> 6);
  const int b = blockIdx.x;
  const int node0 = b * NB;
  int nn = N - node0; nn = nn > NB ? NB : nn; nn = nn < 0 ? 0 : nn;
  const int r = lane & 31, hh = lane >> 5;
  const u32 lds0 = (u32)(uintptr_t)(LAS char*)lds;
  u32* hist = (u32*)(lds + L_HIST);
  u32* off = (u32*)(lds + L_OFF);
  float* macc = (float*)(lds + L_MACC);
  float* pacc = (float*)(lds + L_PACC);
  u32* misc = (u32*)(lds + L_MISC);

  const u32 tv = T[b * NCH + wv * 32 + r];
  const u32x4* we16 = (const u32x4*)we;
  u32x4 wA = we16[tid], wB = we16[512 + tid];
  u32x4 wC = {0, 0, 0, 0};
  if (tid < 40) wC = we16[1024 + tid];
  const u32x4* h16 = (const u32x4*)(h + (size_t)node0 * 64);
  u32x4 hA = {0, 0, 0, 0}, hB = {0, 0, 0, 0};
  if (tid < nn * 8) hA = h16[tid];
  if (tid + 512 < nn * 8) hB = h16[512 + tid];
  u32 pd0 = 0, pd1 = 0;
  if (tid < nn) {
    const u16* p = pos + (size_t)(node0 + tid) * 3;
    pd0 = (u32)p[0] | ((u32)p[1] << 16); pd1 = p[2];
  }
  const float wx0 = bf2f(wx[r]) * NLN2, wx1 = bf2f(wx[32 + r]) * NLN2;

  const u32 cstart = tv & 0xffffu;
  const u32 ccnt = lane < 32 ? (tv >> 16) : 0u;
  const int np = (int)((ccnt + 15) >> 4);
  const int pinc = wave_incl_scan(np);
  const int pexc = pinc - np;
  const int P = __builtin_amdgcn_readlane(pinc, 31);
  const int csum = __builtin_amdgcn_readlane(wave_incl_scan((int)ccnt), 31);

  for (int i = tid; i < 106 * MS; i += 512) macc[i] = 0.f;
  for (int i = tid; i < 106 * 4; i += 512) pacc[i] = 0.f;
  if (tid < 128) *(u32*)(lds + L_ZERO + tid * 4) = 0;
  if (tid < 128) { u32x2 v; v[0] = pd0; v[1] = pd1; *(u32x2*)(lds + L_PD + tid * 8) = v; }
  if (lane == 0) { misc[wv] = (u32)P; misc[8 + wv] = (u32)csum; }
  {
    auto scale8 = [&](u32x4 v) {
      u32x4 o;
#pragma unroll
      for (int i = 0; i < 4; i++) o[i] = pk_bf16(bf2f(v[i] & 0xffffu) * NLOG2E, bf2f(v[i] >> 16) * NLOG2E);
      return o;
    };
    *(u32x4*)(lds + L_WE + tid * 16) = wA;
    *(u32x4*)(lds + L_WE + 8192 + tid * 16) = scale8(wB);
    if (tid < 40) *(u32x4*)(lds + L_WE + 16384 + tid * 16) = wC;
    *(u32x4*)(lds + L_HT + tid * 16) = hA;
    *(u32x4*)(lds + L_HT + 8192 + tid * 16) = hB;
  }
  __syncthreads();

  int Pmax = 0, ntot = 0;
#pragma unroll
  for (int i = 0; i < 8; i++) { Pmax = max(Pmax, (int)misc[i]); ntot += (int)misc[8 + i]; }
  Pmax = __builtin_amdgcn_readfirstlane(Pmax); ntot = __builtin_amdgcn_readfirstlane(ntot);
  const bool normal = (Pmax <= 4 * MAXR) && (ntot <= CAP);
  const int ppb = normal ? 4 * MAXR : 28;
  const int nbatch = normal ? 1 : (Pmax + 27) / 28;

  const int G4 = lane >> 4, i16 = lane & 15, tq = i16 >> 2, tp = i16 & 3, blk = G4 & 1;
  bf16x8 Bw[4][2];
#pragma unroll
  for (int s = 0; s < 4; s++)
#pragma unroll
    for (int ct = 0; ct < 2; ct++) {
      const char* p = lds + L_WE + (64 + 32 * hh + 8 * s + tq) * 128 + (32 * ct + 16 * blk + 4 * tp) * 2;
      Bw[s][ct] = mk8(tr_read(p), tr_read(p + 4 * 128));
    }
  u32 xw[2][2];
#pragma unroll
  for (int ct = 0; ct < 2; ct++) {
    const u16* wt = (const u16*)(lds + L_WE + 16384);
    const int col = 32 * ct + r;
    if (hh == 0) {
      xw[ct][0] = pk_bf16(bf2f(wt[64 + col]) * NLOG2E, bf2f(wt[128 + col]) * NLOG2E);
      xw[ct][1] = pk_bf16(bf2f(wt[192 + col]) * NLOG2E, bf2f(wt[256 + col]) * NLOG2E);
    } else {
      const float wd = bf2f(wt[col]) * NLOG2E;
      const u32 hi = pk_bf16(wd, 0.f) & 0xffffu;
      const u32 lo = pk_bf16(wd - bf2f(hi), 0.f) & 0xffffu;
      xw[ct][0] = hi; xw[ct][1] = lo;
    }
  }
  {
    const int nt = wv >> 1, ct = wv & 1;
    f32x16 acc = {};
#pragma unroll
    for (int s = 0; s < 4; s++) {
      const char* p = lds + L_WE + (32 * hh + 8 * s + tq) * 128 + (32 * ct + 16 * blk + 4 * tp) * 2;
      bf16x8 aw = mk8(tr_read(p), tr_read(p + 4 * 128));
      bf16x8 bh = *(const bf16x8*)(lds + L_HT + (32 * nt + r) * 128 + (4 * hh + s) * 16);
      acc = MFMA(aw, bh, acc);
    }
    const int node = 32 * nt + r;
    if (node < 106) {
#pragma unroll
      for (int q = 0; q < 4; q++) {
        u32x2 v;
        v[0] = pk_bf16(acc[4 * q] * NLOG2E, acc[4 * q + 1] * NLOG2E);
        v[1] = pk_bf16(acc[4 * q + 2] * NLOG2E, acc[4 * q + 3] * NLOG2E);
        *(u32x2*)(lds + L_AP + node * 128 + (32 * ct + 8 * q + 4 * hh) * 2) = v;
      }
    }
  }

  const int kk = lane >> 3, c8 = lane & 7;
  u32 gcst[4];
#pragma unroll
  for (int i = 0; i < 4; i++) gcst[i] = (u32)((c8 ^ i ^ (((kk >> 1) & 1) << 2)) * 16);
  u32 aoff[4];
  {
    const int ii = r & 3, k2 = r >> 2;
#pragma unroll
    for (int s = 0; s < 4; s++) aoff[s] = (u32)(L_GB + wv * 4096 + (ii * 8 + k2) * 128 + (((4 * hh + s) ^ ii ^ (((k2 >> 1) & 1) << 2)) * 16));
  }

  for (int batch = 0; batch < nbatch; batch++) {
    __syncthreads();
    if (tid < 144) hist[tid] = 0;
    u32* desc = (u32*)(lds + L_DESC + wv * 256);
    const int plo = batch * ppb;
    {
      int k0 = plo - pexc; k0 = k0 < 0 ? 0 : k0;
      int k1 = plo + ppb - pexc; k1 = k1 > np ? np : k1;
      const u32 gbase = (u32)((wv * 32 + r) * CH) + cstart;
      for (int k = k0; k < k1; k++) {
        u32 nrec = ccnt - 16 * k; nrec = nrec > 16 ? 16 : nrec;
        desc[pexc + k - plo] = (gbase + 16 * k) | (nrec << 24);
      }
    }
    int Pb = P - plo; Pb = Pb < 0 ? 0 : Pb; Pb = Pb > ppb ? ppb : Pb;
    __syncthreads();
    u32x4 rec[MAXR];
    u32 gv[MAXR];
    bool val[MAXR];
#pragma unroll
    for (int rr = 0; rr < MAXR; rr++) {
      val[rr] = false; gv[rr] = 0; rec[rr] = u32x4{0, 0, 0, 0};
      if (4 * rr < Pb) {
        const int p = 4 * rr + G4;
        const u32 d = p < Pb ? desc[p] : 0u;
        const u32 nrec = d >> 24, gi = (d & 0xffffffu) + i16;
        val[rr] = (u32)i16 < nrec;
        if (val[rr]) { rec[rr] = R[gi]; gv[rr] = G[gi]; }
      }
    }
    u32 rk[MAXR];
#pragma unroll
    for (int rr = 0; rr < MAXR; rr++) {
      rk[rr] = 0;
      if (4 * rr < Pb) { if (val[rr]) rk[rr] = atomicAdd(&hist[gv[rr]], 1u); }
    }
    __syncthreads();
    int n, glast;
    {
      u32x2 c = *(const u32x2*)(hist + 2 * lane);
      int s = c[0] + c[1];
      int inc = wave_incl_scan(s);
      u32x2 o; o[0] = inc - s; o[1] = o[0] + c[0];
      *(u32x2*)(off + 2 * lane) = o;
      n = __builtin_amdgcn_readlane(inc, 63);
      if (lane < 8) { off[128 + lane] = n; off[136 + lane] = n; }
      const unsigned long long m1 = __ballot(c[1] != 0), m0 = __ballot(c[0] != 0);
      const int l1 = m1 ? 63 - __builtin_clzll(m1) : -1, l0 = m0 ? 63 - __builtin_clzll(m0) : -1;
      glast = max(2 * l1 + 1, 2 * l0); glast = glast < 0 ? 0 : glast;
    }
    const int ntiles = (n + 31) >> 5;
#pragma unroll
    for (int rr = 0; rr < MAXR; rr++) {
      if (4 * rr < Pb) {
        if (val[rr]) {
          const u32 g = gv[rr];
          const u32 ps = off[g] + rk[rr];
          const u32x2 pd = *(const u32x2*)(lds + L_PD + g * 8);
          const float dx = bf2f(pd[0] & 0xffffu) - bf2f(rec[rr][2] & 0xffffu);
          const float dy = bf2f(pd[0] >> 16) - bf2f(rec[rr][2] >> 16);
          const float dz = bf2f(pd[1] & 0xffffu) - bf2f(rec[rr][3] & 0xffffu);
          const u32 v0 = pk_bf16(1.0f, dx), v1 = pk_bf16(dy, dz);
          const float rx = bf2f(v0 >> 16), ry = bf2f(v1 & 0xffffu), rz = bf2f(v1 >> 16);
          const u32 d2 = pk_bf16(rx * rx + ry * ry + rz * rz, 0.f) & 0xffffu;
          u32x2 v; v[0] = v0; v[1] = v1;
          *(u32x2*)(lds + L_V + ps * 8) = v;
          u32x2 e; e[0] = rec[rr][0]; e[1] = rec[rr][1];
          *(u32x2*)(lds + L_EA + ps * 8) = e;
          *(u32*)(lds + L_DG + ps * 4) = d2 | (g << 16);
          const u32 pp = (ps & ~31u) | ((ps & 3u) << 3) | ((ps >> 2) & 7u);
          (void)pp;
          *(u16*)(lds + L_SRC + ps * 2) = (u16)(rec[rr][3] >> 16);
        }
      }
    }
    if (tid < 32) {
      const int ps = n + tid;
      if (ps < ntiles * 32) {
        u32x2 z; z[0] = 0; z[1] = 0;
        *(u32x2*)(lds + L_V + ps * 8) = z;
        *(u32x2*)(lds + L_EA + ps * 8) = z;
        *(u32*)(lds + L_DG + ps * 4) = (u32)glast << 16;
        *(u16*)(lds + L_SRC + ps * 2) = 0;
      }
    }
    __syncthreads();

    const int t0 = (ntiles * wv) >> 3, t1 = (ntiles * (wv + 1)) >> 3;
    if (t0 < t1) {
      auto issue = [&](int t) {
        const u32x2 s4 = *(const u32x2*)(lds + L_SRC + (t * 32 + 4 * kk) * 2);
        const u32 gb = lds0 + L_GB + wv * 4096;
        glds16(gb, (s4[0] & 0xffffu) * 128u + gcst[0], h);
        glds16(gb + 1024, (s4[0] >> 16) * 128u + gcst[1], h);
        glds16(gb + 2048, (s4[1] & 0xffffu) * 128u + gcst[2], h);
        glds16(gb + 3072, (s4[1] >> 16) * 128u + gcst[3], h);
      };
      int base = __builtin_amdgcn_readfirstlane((int)(*(const u32*)(lds + L_DG + t0 * 128) >> 16));
      f32x16 Y0 = {}, Y1 = {};
      u32 ap[2][2];
      u32 slo, slen;
      auto load_window = [&](int bs) {
#pragma unroll
        for (int ct = 0; ct < 2; ct++) {
          s16x4 v = tr_read(lds + L_AP + (bs + 4 * hh + tq) * 128 + (32 * ct + 16 * blk + 4 * tp) * 2);
          u32x2 u = __builtin_bit_cast(u32x2, v);
          ap[ct][0] = u[0]; ap[ct][1] = u[1];
        }
        const int nl = bs + 4 * blk + tp;
        slo = off[nl]; slen = off[nl + 1] - slo;
      };
      auto flush = [&](int bs) {
#pragma unroll
        for (int q = 0; q < 4; q++) {
          const int nd = bs + 2 * q + hh;
          atomicAdd(&macc[nd * MS + r], Y0[4 * q]);
          atomicAdd(&macc[nd * MS + 32 + r], Y1[4 * q]);
        }
        float pr[12];
#pragma unroll
        for (int q = 0; q < 4; q++)
#pragma unroll
          for (int c = 1; c < 4; c++) pr[q * 3 + c - 1] = half_reduce(Y0[4 * q + c] * wx0 + Y1[4 * q + c] * wx1);
        if (r == 31) {
#pragma unroll
          for (int q = 0; q < 4; q++)
#pragma unroll
            for (int c = 0; c < 3; c++) atomicAdd(&pacc[(bs + 2 * q + hh) * 4 + c], pr[q * 3 + c]);
        }
#pragma unroll
        for (int i = 0; i < 16; i++) { Y0[i] = 0.f; Y1[i] = 0.f; }
      };
      load_window(base);
      issue(t0);
      for (int t = t0; t < t1; t++) {
        const int Tb = t * 32;
        const u32x2 ea = *(const u32x2*)(lds + L_EA + (Tb + r) * 8);
        const u32 dg = *(const u32*)(lds + L_DG + (Tb + r) * 4);
        asm volatile("s_waitcnt vmcnt(0)" ::: "memory");
        bf16x8 A[4];
#pragma unroll
        for (int s = 0; s < 4; s++) A[s] = *(const bf16x8*)(lds + aoff[s]);
        asm volatile("s_waitcnt lgkmcnt(0)" ::: "memory");
        if (t + 1 < t1) issue(t + 1);
        const int g = (int)(dg >> 16);
        const int gf = __builtin_amdgcn_readlane(g, 0), gl = __builtin_amdgcn_readlane(g, 31);
        if (gl >= base + 8) { flush(base); base = gf; load_window(base); }
        f32x16 X0 = {}, X1 = {};
#pragma unroll
        for (int s = 0; s < 4; s++) { X0 = MFMA(A[s], Bw[s][0], X0); X1 = MFMA(A[s], Bw[s][1], X1); }
        const u32 x01 = hh ? dg : ea[0], x23 = hh ? dg : ea[1];
        for (int wb = base;; wb += 8) {
          const u32 t4 = (u32)(g - wb - 4 * hh);
          unsigned long long oh = 0x3F80ull << ((t4 & 3u) * 16u);
          if (t4 >= 4u) oh = 0ull;
          u32x4 a4; a4[0] = (u32)oh; a4[1] = (u32)(oh >> 32);
          a4[2] = wb == base ? x01 : 0u; a4[3] = wb == base ? x23 : 0u;
          u32x4 b0, b1;
          if (wb != base) load_window(wb);
          b0[0] = ap[0][0]; b0[1] = ap[0][1]; b0[2] = xw[0][0]; b0[3] = xw[0][1];
          b1[0] = ap[1][0]; b1[1] = ap[1][1]; b1[2] = xw[1][0]; b1[3] = xw[1][1];
          X0 = MFMA(__builtin_bit_cast(bf16x8, a4), __builtin_bit_cast(bf16x8, b0), X0);
          X1 = MFMA(__builtin_bit_cast(bf16x8, a4), __builtin_bit_cast(bf16x8, b1), X1);
          if (gl < wb + 8) { if (wb != base) load_window(base); break; }
        }
        u32x4 M[2][2];
#pragma unroll
        for (int s = 0; s < 2; s++)
#pragma unroll
          for (int j = 0; j < 4; j++) {
            M[0][s][j] = silu2(X0[8 * s + 2 * j], X0[8 * s + 2 * j + 1]);
            M[1][s][j] = silu2(X1[8 * s + 2 * j], X1[8 * s + 2 * j + 1]);
          }
        for (;;) {
          const u32 d0 = (u32)(Tb + 4 * hh + tq) - slo;
          const char* vp = lds + L_V + (Tb + 4 * hh + tq) * 8;
          const char* zp = lds + L_ZERO;
          s16x4 sf[4];
#pragma unroll
          for (int u = 0; u < 4; u++) sf[u] = tr_read(((d0 + 8u * u) < slen ? vp : zp) + 64 * u);
#pragma unroll
          for (int s = 0; s < 2; s++) {
            const bf16x8 S = mk8(sf[2 * s], sf[2 * s + 1]);
            Y0 = MFMA(S, __builtin_bit_cast(bf16x8, M[0][s]), Y0);
            Y1 = MFMA(S, __builtin_bit_cast(bf16x8, M[1][s]), Y1);
          }
          if (gl < base + 8) break;
          flush(base); base += 8; load_window(base);
        }
      }
      flush(base);
    }
  }
  __syncthreads();

  constexpr int L_WH = L_V;
  constexpr int L_H2 = L_V + 16384;
  constexpr int L_UT = L_V + 32768;
  {
    const u32x4* wh16 = (const u32x4*)wh;
    *(u32x4*)(lds + L_WH + tid * 16) = wh16[tid];
    *(u32x4*)(lds + L_WH + 8192 + tid * 16) = wh16[512 + tid];
    u32x4 z = {0, 0, 0, 0};
    *(u32x4*)(lds + L_H2 + tid * 16) = tid < nn * 8 ? h16[tid] : z;
    *(u32x4*)(lds + L_H2 + 8192 + tid * 16) = (tid + 512) < nn * 8 ? h16[512 + tid] : z;
  }
  __syncthreads();
  {
    const int nt = wv >> 1, ct = wv & 1;
    const int node = 32 * nt + r;
    const int nrow = node < 106 ? node : 105;
    f32x16 acc = {};
#pragma unroll
    for (int s = 0; s < 8; s++) {
      const int kb = (s < 4) ? (32 * hh + 8 * s) : (64 + 32 * hh + 8 * (s - 4));
      const char* p = lds + L_WH + (kb + tq) * 128 + (32 * ct + 16 * blk + 4 * tp) * 2;
      const bf16x8 aw = mk8(tr_read(p), tr_read(p + 4 * 128));
      bf16x8 bb;
      if (s < 4) {
        bb = *(const bf16x8*)(lds + L_H2 + node * 128 + (4 * hh + s) * 16);
      } else {
        const float* mp = macc + nrow * MS + 32 * hh + 8 * (s - 4);
        const f32x4 m0 = *(const f32x4*)mp, m1 = *(const f32x4*)(mp + 4);
        u32x4 pk;
        pk[0] = pk_bf16(m0[0] * NLN2, m0[1] * NLN2); pk[1] = pk_bf16(m0[2] * NLN2, m0[3] * NLN2);
        pk[2] = pk_bf16(m1[0] * NLN2, m1[1] * NLN2); pk[3] = pk_bf16(m1[2] * NLN2, m1[3] * NLN2);
        bb = __builtin_bit_cast(bf16x8, pk);
      }
      acc = MFMA(aw, bb, acc);
    }
#pragma unroll
    for (int q = 0; q < 4; q++) {
      float u[4];
#pragma unroll
      for (int i = 0; i < 4; i++) {
        const float x = bf2f(pk_bf16(acc[4 * q + i], 0.f) & 0xffffu);
        u[i] = x * __builtin_amdgcn_rcpf(1.0f + __builtin_amdgcn_exp2f(x * NLOG2E));
      }
      u32x2 v; v[0] = pk_bf16(u[0], u[1]); v[1] = pk_bf16(u[2], u[3]);
      *(u32x2*)(lds + L_UT + node * 144 + (32 * ct + 8 * q + 4 * hh) * 2) = v;
    }
  }
  __syncthreads();
  for (int i = tid; i < nn * 8; i += 512) {
    const int row = i >> 3, chn = i & 7;
    const u32x4 u = *(const u32x4*)(lds + L_UT + row * 144 + chn * 16);
    const u32x4 hv = *(const u32x4*)(lds + L_H2 + i * 16);
    u32x4 o;
#pragma unroll
    for (int j = 0; j < 4; j++)
      o[j] = pk_bf16(bf2f(hv[j] & 0xffffu) + bf2f(u[j] & 0xffffu), bf2f(hv[j] >> 16) + bf2f(u[j] >> 16));
    *(u32x4*)(out + (size_t)(node0 + row) * 64 + chn * 8) = o;
  }
  if (tid < nn * 3) {
    const int nd = tid / 3, c = tid - nd * 3;
    const u16 pv = pos[(size_t)node0 * 3 + tid];
    const float dlt = bf2f(pk_bf16(pacc[nd * 4 + c], 0.f) & 0xffffu);
    out[(size_t)N * 64 + (size_t)node0 * 3 + tid] = (u16)(pk_bf16(bf2f(pv) + dlt, 0.f) & 0xffffu);
  }
}

extern "C" void kernel_launch(void* const* d_in, const int* in_sizes, int n_in, void* d_out, int out_size, void* d_ws,
                              size_t ws_size, hipStream_t stream) {
  const u16* h = (const u16*)d_in[0];
  const u16* pos = (const u16*)d_in[1];
  const u16* ea = (const u16*)d_in[2];
  const int* src = (const int*)d_in[3];
  const int* dst = (const int*)d_in[4];
  const u16* we = (const u16*)d_in[5];
  const u16* wx = (const u16*)d_in[6];
  const u16* wh = (const u16*)d_in[7];
  const int N = in_sizes[0] / 64, E = in_sizes[3];
  char* ws = (char*)d_ws;
  u32x4* R = (u32x4*)ws;
  u8* G = (u8*)(ws + (size_t)NCH * CH * 16);
  u32* T = (u32*)(ws + (size_t)NCH * CH * 16 + 1048576);
  (void)hipFuncSetAttribute((const void*)k1_partition, hipFuncAttributeMaxDynamicSharedMemorySize, K1_LDS);
  (void)hipFuncSetAttribute((const void*)k2_main, hipFuncAttributeMaxDynamicSharedMemorySize, K2_LDS);
  k1_partition<<<NCH, 1024, K1_LDS, stream>>>(pos, (const u32x4*)ea, (const i32x4*)src, (const i32x4*)dst, R, G, T, N, E);
  k2_main<<<NBUCK, 512, K2_LDS, stream>>>(h, pos, we, wx, wh, R, G, T, (u16*)d_out, N);
}
```

```cpp
#include <hip/hip_runtime.h>
#include <stdint.h>
#include <type_traits>

typedef unsigned short u16;
typedef unsigned char u8;
typedef unsigned int u32;
typedef short s16x4 __attribute__((ext_vector_type(4)));
typedef __bf16 bf16x2 __attribute__((ext_vector_type(2)));
typedef __bf16 bf16x8 __attribute__((ext_vector_type(8)));
typedef float f32x2 __attribute__((ext_vector_type(2)));
typedef float f32x4 __attribute__((ext_vector_type(4)));
typedef float f32x16 __attribute__((ext_vector_type(16)));
typedef u32 u32x2 __attribute__((ext_vector_type(2)));
typedef u32 u32x3 __attribute__((ext_vector_type(3)));
typedef u32 u32x4 __attribute__((ext_vector_type(4)));
typedef int i32x4 __attribute__((ext_vector_type(4)));
#define LAS __attribute__((address_space(3)))
#define DI __device__ __forceinline__
#define AI __attribute__((always_inline))

constexpr int NB    = 98;
constexpr int CH    = 3128;
constexpr int NCH   = 256;
constexpr int CAP   = 3584;
constexpr int MAXR  = 12;
constexpr int FPPB  = 28;
constexpr float NLOG2E = -1.4426950408889634f;
constexpr float NLN2   = -0.6931471805599453f;

DI float bf2f(u32 bits16) { return __uint_as_float(bits16 << 16); }
DI float bflo(u32 v) { return __uint_as_float(v << 16); }
DI float bfhi(u32 v) { return __uint_as_float(v & 0xffff0000u); }
DI u32 pk_bf16(float lo, float hi) {
  f32x2 v; v[0] = lo; v[1] = hi;
  return __builtin_bit_cast(u32, __builtin_convertvector(v, bf16x2));
}
template <int CTRL, int RMASK> DI int dpp_i(int x) { return __builtin_amdgcn_update_dpp(0, x, CTRL, RMASK, 0xf, false); }
template <int CTRL> DI float dpp_f(float x) {
  return __builtin_bit_cast(float, __builtin_amdgcn_update_dpp(0, __builtin_bit_cast(int, x), CTRL, 0xf, 0xf, true));
}
DI int wave_incl_scan(int x) {
  x += dpp_i<0x111, 0xf>(x); x += dpp_i<0x112, 0xf>(x); x += dpp_i<0x114, 0xf>(x); x += dpp_i<0x118, 0xf>(x);
  x += dpp_i<0x142, 0xa>(x); x += dpp_i<0x143, 0xc>(x);
  return x;
}
DI float half_reduce(float x) {
  x += dpp_f<0x111>(x); x += dpp_f<0x112>(x); x += dpp_f<0x114>(x); x += dpp_f<0x118>(x);
  x += dpp_f<0x142>(x);
  return x;
}
DI void glds16(u32 lds_addr, u32 voff, const void* base) {
  asm volatile("s_mov_b32 m0, %0\n\ts_nop 0\n\tglobal_load_lds_dwordx4 %1, %2" ::"s"(lds_addr), "v"(voff), "s"(base) : "memory");
}
DI void glds16_lo32(u32 lds_addr, u32 voff, const void* base) {
  asm volatile("s_mov_b32 m0, %0\n\ts_mov_b64 exec, 0xffffffff\n\tglobal_load_lds_dwordx4 %1, %2\n\ts_mov_b64 exec, -1"
               ::"s"(lds_addr), "v"(voff), "s"(base) : "memory");
}
template <class T> DI T ldl(u32 a) { return *(const LAS T*)(uintptr_t)a; }
template <class T> DI void stl(u32 a, T v) { *(LAS T*)(uintptr_t)a = v; }
DI s16x4 trl(u32 a) { return __builtin_amdgcn_ds_read_tr16_b64_v4i16((LAS s16x4*)(uintptr_t)a); }
DI bf16x8 mk8(s16x4 lo, s16x4 hi) {
  return __builtin_bit_cast(bf16x8, __builtin_shufflevector(lo, hi, 0, 1, 2, 3, 4, 5, 6, 7));
}
#define BAR() do { asm volatile("s_waitcnt lgkmcnt(0)" ::: "memory"); __builtin_amdgcn_s_barrier(); asm volatile("" ::: "memory"); } while (0)
#define SB() __builtin_amdgcn_sched_barrier(0)
#define PIN(x) asm volatile("" : "+v"(x))
#define STAMP(k,i)
#define STAMP_DECL
#define STAMP_OUT(k,base)
template <int I, int N, class F> DI void static_for(F&& f) {
  if constexpr (I < N) { f(std::integral_constant<int, I>{}); static_for<I + 1, N>(f); }
}
#define MFMA(a, b, c) __builtin_amdgcn_mfma_f32_32x32x16_bf16((a), (b), (c), 0, 0, 0)
#define BF8(x) __builtin_bit_cast(bf16x8, (x))

constexpr int K1_HIST = CH * 12 + 64;
constexpr int K1_OFF = K1_HIST + 1024 + 64;
constexpr int K1_LDS = K1_OFF + 1024 + 64;

__global__ __launch_bounds__(1024) void k1_partition(const u16* __restrict__ pos, const u32x4* __restrict__ ea4,
                                                     const i32x4* __restrict__ src4, const i32x4* __restrict__ dst4,
                                                     u32* __restrict__ R, f32x4* __restrict__ P4, u32* __restrict__ T,
                                                     int N, int E) {
  extern __shared__ __attribute__((aligned(16))) char lds[];
  const int tid = threadIdx.x, lane = tid & 63;
  const int wv = __builtin_amdgcn_readfirstlane(tid >> 6);
  const int w = blockIdx.x;
  u32* hist = (u32*)(lds + K1_HIST);
  u32* off = (u32*)(lds + K1_OFF);
  STAMP_DECL
  STAMP(0, 0);
  int cnt = E - w * CH; cnt = cnt > CH ? CH : cnt; cnt = cnt < 4 ? 4 : cnt;
  const bool act = 4 * tid < cnt;
  const int q = (w * CH >> 2) + (act ? tid : 0);
  const i32x4 d4 = dst4[q];
  const i32x4 s4 = src4[q];
  const u32x4 a0 = ea4[2 * q];
  const u32x4 a1 = ea4[2 * q + 1];
  {
    const int n = w * NB + (tid - 896);
    if (tid >= 896 && tid < 896 + NB && n < N) {
      const u16* p = pos + (size_t)n * 3;
      f32x4 v; v[0] = bf2f(p[0]); v[1] = bf2f(p[1]); v[2] = bf2f(p[2]); v[3] = 0.f;
      P4[n] = v;
    }
  }
  if (tid < 272) hist[tid] = 0;
  BAR();
  STAMP(0, 1);
  int bk[4], gl[4], rk[4];
#pragma unroll
  for (int i = 0; i < 4; i++) {
    const u32 d = (u32)d4[i];
    bk[i] = (int)((d * 42800u) >> 22);
    gl[i] = (int)d - bk[i] * NB;
    bk[i] = act ? bk[i] : 256 + (lane & 15);
    rk[i] = atomicAdd(&hist[bk[i]], 1u);
  }
  STAMP(0, 2);
  BAR();
  STAMP(0, 3);
  {
    u32x4 c = *(const u32x4*)(hist + 4 * lane);
    int s = c[0] + c[1] + c[2] + c[3];
    int inc = wave_incl_scan(s);
    u32x4 o;
    o[0] = inc - s; o[1] = o[0] + c[0]; o[2] = o[1] + c[1]; o[3] = o[2] + c[2];
    *(u32x4*)(off + 4 * lane) = o;
    if (wv == 15) {
#pragma unroll
      for (int i = 0; i < 4; i++) T[(4 * lane + i) * NCH + w] = o[i] | (c[i] << 16);
    }
  }
  STAMP(0, 4);
  if (act) {
#pragma unroll
    for (int i = 0; i < 4; i++) {
      const int slot = (int)(off[bk[i]] + rk[i]);
      u32* p = (u32*)(lds + slot * 12);
      p[0] = i < 2 ? a0[2 * i] : a1[2 * i - 4];
      p[1] = i < 2 ? a0[2 * i + 1] : a1[2 * i - 3];
      p[2] = (u32)s4[i] | ((u32)gl[i] << 16);
    }
  }
  BAR();
  STAMP(0, 5);
  u32x4* Rw = (u32x4*)(R + (size_t)w * CH * 3);
  const int n16 = (cnt * 12 + 15) >> 4;
#pragma unroll
  for (int k = 0; k < 3; k++) {
    const int i = tid + 1024 * k;
    if (i < n16) Rw[i] = *(const u32x4*)(lds + i * 16);
  }
  STAMP(0, 6);
  STAMP_OUT(0, R);
}

constexpr int L_GB = 0;
constexpr int L_PB = L_GB + 8 * 4096;
constexpr int L_VB = L_PB + 8 * 512;
constexpr int L_EA = L_VB + 8 * 256;
constexpr int L_SG = L_EA + (CAP + 32) * 8;
constexpr int L_AP = L_SG + (CAP + 96) * 4;
constexpr int MS = 68;
constexpr int MDUMMY = 98;
constexpr int L_MACC = L_AP + 106 * 128;
constexpr int L_HB = L_MACC + 99 * MS * 4;
constexpr int L_PACC = L_HB + 8 * MS * 4;
constexpr int L_HIST = L_PACC + 106 * 16;
constexpr int L_OFF = L_HIST + 144 * 4;
constexpr int L_ZERO = L_OFF + 144 * 4;
constexpr int L_PD = L_ZERO + 256;
constexpr int L_MISC = L_PD + 128 * 16;
constexpr int L_WH = L_MISC + 256;
constexpr int L_H2 = L_WH + 16384;
constexpr int K2_LDS = L_H2 + 16384;
static_assert(K2_LDS <= 163840, "LDS");
static_assert(L_AP % 16 == 0 && L_MACC % 16 == 0 && L_EA % 16 == 0 && L_SG % 16 == 0 && L_HB % 16 == 0 && L_PD % 16 == 0 && L_WH % 16 == 0, "align");
constexpr int L_WE = L_EA;
constexpr int L_DESC = L_GB;
constexpr int L_UT = L_EA;

DI u32 silu2(float z0, float z1) {
  f32x2 z; z[0] = z0; z[1] = z1;
  f32x2 t; t[0] = __builtin_amdgcn_exp2f(z0); t[1] = __builtin_amdgcn_exp2f(z1);
  t = t + 1.0f;
  f32x2 r; r[0] = __builtin_amdgcn_rcpf(t[0]); r[1] = __builtin_amdgcn_rcpf(t[1]);
  f32x2 m = z * r;
  return __builtin_bit_cast(u32, __builtin_convertvector(m, bf16x2));
}

__global__ __launch_bounds__(512) void k2_main(const u16* __restrict__ h, const u16* __restrict__ pos,
                                               const u16* __restrict__ we, const u16* __restrict__ wx,
                                               const u16* __restrict__ wh, const u32* __restrict__ R,
                                               const f32x4* __restrict__ P4, const u32* __restrict__ T,
                                               u16* __restrict__ out, int N) {
  extern __shared__ __attribute__((aligned(16))) char lds[];
  const int tid = threadIdx.x, lane = tid & 63;
  const int wv = __builtin_amdgcn_readfirstlane(tid >> 6);
  const int b = blockIdx.x;
  const int node0 = b * NB;
  int nn = N - node0; nn = nn > NB ? NB : nn; nn = nn < 1 ? 1 : nn;
  const int r = lane & 31, hh = lane >> 5;
  const u32 lds0 = (u32)(uintptr_t)(LAS char*)lds;
  u32* hist = (u32*)(lds + L_HIST);
  u32* off = (u32*)(lds + L_OFF);
  float* macc = (float*)(lds + L_MACC);
  float* pacc = (float*)(lds + L_PACC);
  u32* misc = (u32*)(lds + L_MISC);
  STAMP_DECL
  STAMP(1, 0);

  const u32 tv = T[b * NCH + wv * 32 + r];
  const u32x4* we16 = (const u32x4*)we;
  const u32x4 wA = we16[tid], wB = we16[512 + tid];
  const u32x4 wC = we16[1024 + (tid < 40 ? tid : 0)];
  const u32x4* h16 = (const u32x4*)(h + (size_t)node0 * 64);
  const u32x4 hA = h16[tid < nn * 8 ? tid : 0];
  const u32x4 hB = h16[tid + 512 < nn * 8 ? tid + 512 : 0];
  const u32x4* wh16 = (const u32x4*)wh;
  const u32x4 uA = wh16[tid], uB = wh16[512 + tid];
  const u16* pp = pos + (size_t)(node0 + (tid < nn ? tid : 0)) * 3;
  const u32 pd0 = pp[0], pd1 = pp[1], pd2 = pp[2];
  const float wx0 = bf2f(wx[r]) * NLN2, wx1 = bf2f(wx[32 + r]) * NLN2;

  const u32 cstart = tv & 0xffffu;
  const u32 ccnt = lane < 32 ? (tv >> 16) : 0u;
  const int np = (int)((ccnt + 15) >> 4);
  const int pinc = wave_incl_scan(np);
  const int pexc = pinc - np;
  const int P = __builtin_amdgcn_readlane(pinc, 31);
  const int csum = __builtin_amdgcn_readlane(wave_incl_scan((int)ccnt), 31);
  STAMP(1, 1);

  const int G4 = lane >> 4, i16 = lane & 15, tq = i16 >> 2, tp = i16 & 3, blk = G4 & 1;
  bf16x8 Bw[4][2];
  u32 xw[2][2];
  u32x3 rec[MAXR];
  u32 vmask = 0;
  u32* desc = (u32*)(lds + L_DESC + wv * 256);
  auto load_records = [&](int plo, int ppb) AI {
    int k0 = plo - pexc; k0 = k0 < 0 ? 0 : k0;
    int k1 = plo + ppb - pexc; k1 = k1 > np ? np : k1;
    const u32 gbase = (u32)((wv * 32 + r) * CH) + cstart;
    for (int k = k0; k < k1; k++) {
      u32 nrec = ccnt - 16 * k; nrec = nrec > 16 ? 16 : nrec;
      desc[pexc + k - plo] = (gbase + 16 * k) | (nrec << 24);
    }
    int Pb = P - plo; Pb = Pb < 0 ? 0 : Pb; Pb = Pb > ppb ? ppb : Pb;
    u32 dd[MAXR];
#pragma unroll
    for (int rr = 0; rr < MAXR; rr++) dd[rr] = desc[4 * rr + G4];
    vmask = 0;
#pragma unroll
    for (int rr = 0; rr < MAXR; rr++) {
      const u32 d = (4 * rr + G4 < Pb) ? dd[rr] : 0u;
      const bool v = (u32)i16 < (d >> 24);
      const u32 gi = (d & 0xffffffu) + (v ? i16 : 0);
      rec[rr] = *(const u32x3*)(R + (size_t)gi * 3);
      vmask |= v ? (1u << rr) : 0u;
    }
  };
  auto process = [&](auto fast_tag) AI {
    constexpr bool ALLOW_FAST = decltype(fast_tag)::value;
    STAMP(1, 4);
    u32 rk[MAXR];
#pragma unroll
    for (int rr = 0; rr < MAXR; rr++) {
      const u32 g = (vmask >> rr) & 1u ? (rec[rr][2] >> 16) : 128u + (lane & 15);
      rk[rr] = atomicAdd(&hist[g], 1u);
    }
    BAR();
    STAMP(1, 5);
    int n, glast;
    {
      u32x2 c = *(const u32x2*)(hist + 2 * lane);
      int s = c[0] + c[1];
      int inc = wave_incl_scan(s);
      u32x2 o; o[0] = inc - s; o[1] = o[0] + c[0];
      *(u32x2*)(off + 2 * lane) = o;
      n = __builtin_amdgcn_readlane(inc, 63);
      if (lane < 16) off[128 + lane] = n;
      const unsigned long long m1 = __ballot(c[1] != 0), m0 = __ballot(c[0] != 0);
      const int l1 = m1 ? 63 - __builtin_clzll(m1) : -1, l0 = m0 ? 63 - __builtin_clzll(m0) : -1;
      glast = max(2 * l1 + 1, 2 * l0); glast = glast < 0 ? 0 : glast;
    }
    const int ntiles = (n + 31) >> 5;
#pragma unroll
    for (int rr = 0; rr < MAXR; rr++) {
      const bool v = (vmask >> rr) & 1u;
      const u32 g = v ? (rec[rr][2] >> 16) : 0u;
      const u32 ps = v ? off[g] + rk[rr] : (u32)(CAP + (lane & 31));
      u32x2 e; e[0] = rec[rr][0]; e[1] = rec[rr][1];
      *(u32x2*)(lds + L_EA + ps * 8) = e;
      *(u32*)(lds + L_SG + (v ? ps : ps + 64u) * 4) = rec[rr][2];
    }
    {
      const int ps = n + lane + 64 * (wv & 1);
      if (wv >= 6 && ps < ntiles * 32 + 64) {
        u32x2 z; z[0] = 0; z[1] = 0;
        if (ps < ntiles * 32) *(u32x2*)(lds + L_EA + ps * 8) = z;
        *(u32*)(lds + L_SG + ps * 4) = ((u32)glast << 16) | 0x8000u;
      }
    }
    BAR();
    STAMP(1, 6);

    int ln = lane;
    asm volatile("" : "+v"(ln));
    const int r = ln & 31, hh = ln >> 5;
    const int G4 = ln >> 4, i16 = ln & 15, tq = i16 >> 2, tp = i16 & 3, blk = G4 & 1;
    const int kk = ln >> 3, c8 = ln & 7;
    u32 gcst[4], aoff[4];
#pragma unroll
    for (int i = 0; i < 4; i++) gcst[i] = (u32)((c8 ^ i ^ (((kk >> 1) & 1) << 2)) * 16);
    {
      const int ii = r & 3, k2 = r >> 2;
#pragma unroll
      for (int s = 0; s < 4; s++)
        aoff[s] = lds0 + (u32)(L_GB + wv * 4096 + (ii * 8 + k2) * 128 + (((4 * hh + s) ^ ii ^ (((k2 >> 1) & 1) << 2)) * 16));
    }
    const int t0 = (ntiles * wv) >> 3, t1 = (ntiles * (wv + 1)) >> 3;
    int head = 127;
    if (t0 < t1) {
      const u32 gb = lds0 + L_GB + wv * 4096;
      const u32 pb = lds0 + L_PB + wv * 512;
      const u32 vb = lds0 + L_VB + wv * 256;
      const u32 a_ps = pb + r * 16;
      const u32 a_vw = vb + r * 8;
      const u32 a_vr = vb + (4 * hh + tq) * 8;
      const u32 a_z = lds0 + L_ZERO;
      auto sg_read = [&](u32 a_k, u32 a_r, u32x4& sk, u32& sr) AI {
        sk[0] = ldl<u32>(a_k); sk[1] = ldl<u32>(a_k + 4); sk[2] = ldl<u32>(a_k + 8); sk[3] = ldl<u32>(a_k + 12);
        sr = ldl<u32>(a_r);
      };
      auto issue = [&](const u32x4& sk, u32 sr) AI {
        glds16(gb, (sk[0] & 0x7fffu) * 128u + gcst[0], h);
        glds16(gb + 1024, (sk[1] & 0x7fffu) * 128u + gcst[1], h);
        glds16(gb + 2048, (sk[2] & 0x7fffu) * 128u + gcst[2], h);
        glds16(gb + 3072, (sk[3] & 0x7fffu) * 128u + gcst[3], h);
        glds16_lo32(pb, (sr & 0x7fffu) * 16u, P4);
      };
      u32 a_sk = lds0 + (u32)(L_SG + (t0 * 32 + 4 * kk) * 4), a_sr = lds0 + (u32)(L_SG + (t0 * 32 + r) * 4);
      u32 sg_cur;
      { u32x4 sk; sg_read(a_sk, a_sr, sk, sg_cur); issue(sk, sg_cur); a_sk += 128; a_sr += 128; }
      const int tl = (t0 + ln < t1) ? t0 + ln : t1 - 1;
      const int gfv = (int)(*(const u32*)(lds + L_SG + tl * 128) >> 16);
      const int glv = (int)(*(const u32*)(lds + L_SG + tl * 128 + 124) >> 16);
      const bool wide = __ballot(glv - gfv >= 8) != 0ull || (t1 - t0) > 64;
      int base = __builtin_amdgcn_readlane(gfv, 0);
      if ((int)__builtin_amdgcn_readfirstlane(off[base]) < t0 * 32) head = base;
      const int gspan = __builtin_amdgcn_readfirstlane((int)(*(const u32*)(lds + L_SG + (t1 * 32 - 1) * 4) >> 16));
      f32x16 Y0 = {}, Y1 = {};
      u32 ap[2][2];
      u32 slo, slen;
      auto load_ap = [&](int bs) AI {
#pragma unroll
        for (int ct = 0; ct < 2; ct++) {
          s16x4 v = trl(lds0 + (u32)(L_AP + (bs + 4 * hh + tq) * 128 + (32 * ct + 16 * blk + 4 * tp) * 2));
          u32x2 u = __builtin_bit_cast(u32x2, v);
          ap[ct][0] = u[0]; ap[ct][1] = u[1];
        }
      };
      auto load_sel = [&](int bs) AI {
        const int nl = bs + 4 * blk + tp;
        slo = off[nl]; slen = off[nl + 1] - slo;
      };
      auto flush = [&](int bs) AI {
        u32 ma[4];
        float o0[4], o1[4];
#pragma unroll
        for (int q = 0; q < 4; q++) {
          const int nd = bs + 2 * q + hh;
          u32 a = (u32)(L_MACC + nd * (MS * 4));
          a = nd > gspan ? (u32)(L_MACC + MDUMMY * (MS * 4)) : a;
          a = nd == head ? (u32)(L_HB + wv * (MS * 4)) : a;
          ma[q] = lds0 + a + 4 * r;
        }
#pragma unroll
        for (int q = 0; q < 4; q++) { o0[q] = ldl<float>(ma[q]); o1[q] = ldl<float>(ma[q] + 128); }
        SB();
#pragma unroll
        for (int q = 0; q < 4; q++) { stl<float>(ma[q], o0[q] + Y0[4 * q]); stl<float>(ma[q] + 128, o1[q] + Y1[4 * q]); }
        float pr[12];
#pragma unroll
        for (int q = 0; q < 4; q++)
#pragma unroll
          for (int c = 1; c < 4; c++) pr[q * 3 + c - 1] = half_reduce(Y0[4 * q + c] * wx0 + Y1[4 * q + c] * wx1);
        if (r == 31) {
#pragma unroll
          for (int q = 0; q < 4; q++)
#pragma unroll
            for (int c = 0; c < 3; c++) atomicAdd(&pacc[(bs + 2 * q + hh) * 4 + c], pr[q * 3 + c]);
        }
#pragma unroll
        for (int i = 0; i < 16; i++) { Y0[i] = 0.f; Y1[i] = 0.f; }
      };
      auto geom = [&](u32 sg, const f32x4& ps, const f32x4& pd) AI {
        const float dx = pd[0] - ps[0], dy = pd[1] - ps[1], dz = pd[2] - ps[2];
        u32x2 v; v[0] = pk_bf16(1.0f, dx); v[1] = pk_bf16(dy, dz);
        const float rx = bfhi(v[0]), ry = bflo(v[1]), rz = bfhi(v[1]);
        const u32 d2 = pk_bf16(rx * rx + ry * ry + rz * rz, 0.f);
        if (sg & 0x8000u) { v[0] = 0; v[1] = 0; }
        stl<u32x2>(a_vw, v);
        return d2;
      };
      auto make_a4 = [&](u32 sg, u32 d2, u32 ea0, u32 ea1, int wb, bool with_x) AI {
        const u32 t4 = (sg >> 16) - (u32)(wb + 4 * hh);
        unsigned long long oh = 0x3F80ull << ((t4 & 3u) * 16u);
        if (t4 >= 4u) oh = 0ull;
        u32x4 a4; a4[0] = (u32)oh; a4[1] = (u32)(oh >> 32);
        a4[2] = with_x ? (hh ? d2 : ea0) : 0u; a4[3] = with_x ? (hh ? d2 : ea1) : 0u;
        return a4;
      };

      if (ALLOW_FAST && !wide) {
        int base1 = base, base2 = base;
        load_ap(base1);
        load_sel(base2);
        f32x16 XA0, XA1, XB0, XB1;
        u32 a_ea = lds0 + (u32)(L_EA + (t0 * 32 + r) * 8);
        u32 e_s = (u32)(t0 * 32 + 4 * hh + tq);
        const u32 a_pd = lds0 + L_PD;
        {
          const u32x2 ea = ldl<u32x2>(a_ea);
          const f32x4 pd = ldl<f32x4>(a_pd + (sg_cur >> 16) * 16);
          u32x4 sk; u32 sr; sg_read(a_sk, a_sr, sk, sr);
          asm volatile("s_waitcnt vmcnt(0)" ::: "memory");
          bf16x8 A[4];
#pragma unroll
          for (int s = 0; s < 4; s++) A[s] = ldl<bf16x8>(aoff[s]);
          const f32x4 ps = ldl<f32x4>(a_ps);
          asm volatile("s_waitcnt lgkmcnt(0)" ::: "memory");
          issue(sk, sr);
          const u32 d2 = geom(sg_cur, ps, pd);
          XA0 = f32x16{}; XA1 = f32x16{};
#pragma unroll
          for (int s = 0; s < 4; s++) { XA0 = MFMA(A[s], Bw[s][0], XA0); XA1 = MFMA(A[s], Bw[s][1], XA1); }
          const u32x4 a4 = make_a4(sg_cur, d2, ea[0], ea[1], base1, true);
          u32x4 b0, b1;
          b0[0] = ap[0][0]; b0[1] = ap[0][1]; b0[2] = xw[0][0]; b0[3] = xw[0][1];
          b1[0] = ap[1][0]; b1[1] = ap[1][1]; b1[2] = xw[1][0]; b1[3] = xw[1][1];
          XA0 = MFMA(BF8(a4), BF8(b0), XA0); XA1 = MFMA(BF8(a4), BF8(b1), XA1);
          sg_cur = sr; a_sk += 128; a_sr += 128; a_ea += 256;
          asm volatile("s_nop 7\n\ts_nop 7" : "+v"(XA0), "+v"(XA1));
        }
        auto step = [&](auto next_tag, f32x16& C0, f32x16& C1, f32x16& N0, f32x16& N1, int t) AI {
          constexpr bool NEXT = decltype(next_tag)::value;
          if (NEXT) {
            const int gl1 = __builtin_amdgcn_readlane(glv, t + 1 - t0), gf1 = __builtin_amdgcn_readlane(gfv, t + 1 - t0);
            if (gl1 >= base1 + 8) { base1 = gf1; load_ap(base1); }
          }
          u32x4 M[2][2];
          bf16x8 A[4]; u32x2 ea; u32x4 sk; u32 sr; f32x4 ps, pd;
          s16x4 sf[4];
          u32x4 a4, b0, b1;
          f32x2 tt[16];
          SB();
#define XP(p) (((p) >> 2) & 1 ? __builtin_shufflevector(C1, C1, 8 * ((p) >> 3) + 2 * ((p) & 3), 8 * ((p) >> 3) + 2 * ((p) & 3) + 1) \
                              : __builtin_shufflevector(C0, C0, 8 * ((p) >> 3) + 2 * ((p) & 3), 8 * ((p) >> 3) + 2 * ((p) & 3) + 1))
          static_for<0, 18>([&](auto ic) AI {
            constexpr int i = decltype(ic)::value;
            if constexpr (i == 0) {
              if constexpr (NEXT) {
                asm volatile("s_waitcnt vmcnt(0)" ::: "memory");
#pragma unroll
                for (int s = 0; s < 4; s++) A[s] = ldl<bf16x8>(aoff[s]);
                ps = ldl<f32x4>(a_ps);
                pd = ldl<f32x4>(a_pd + (sg_cur >> 16) * 16);
                ea = ldl<u32x2>(a_ea);
                sg_read(a_sk, a_sr, sk, sr);
              }
              const u32 d0 = e_s - slo;
#pragma unroll
              for (int u = 0; u < 4; u++) sf[u] = trl(((d0 + 8u * u) < slen ? a_vr : a_z) + 64 * u);
              SB();
            }
            if constexpr (i == 3 && NEXT) {
              asm volatile("s_waitcnt lgkmcnt(0)" ::: "memory");
              issue(sk, sr);
              SB();
            }
            if constexpr (i == 5 && NEXT) {
              const u32 d2 = geom(sg_cur, ps, pd);
              a4 = make_a4(sg_cur, d2, ea[0], ea[1], base1, true);
              b0[0] = ap[0][0]; b0[1] = ap[0][1]; b0[2] = xw[0][0]; b0[3] = xw[0][1];
              b1[0] = ap[1][0]; b1[1] = ap[1][1]; b1[2] = xw[1][0]; b1[3] = xw[1][1];
              SB();
            }
            if constexpr (i == 0) {
              const f32x2 x = XP(0);
              asm volatile("v_exp_f32 %0, %2\n\tv_exp_f32 %1, %3" : "=&v"(tt[0][0]), "=&v"(tt[0][1]) : "v"(x[0]), "v"(x[1]));
            } else if constexpr (i == 1) {
              const f32x2 x = XP(1);
              asm volatile("v_exp_f32 %0, %3\n\tv_exp_f32 %1, %4\n\tv_pk_add_f32 %2, %2, 1.0 op_sel_hi:[1,0]"
                           : "=&v"(tt[1][0]), "=&v"(tt[1][1]), "+v"(tt[0]) : "v"(x[0]), "v"(x[1]));
            } else if constexpr (i < 16) {
              const f32x2 x = XP(i < 16 ? i : 0), xm = XP(i - 2);
              asm volatile("v_exp_f32 %0, %4\n\tv_exp_f32 %1, %5\n\tv_pk_add_f32 %2, %2, 1.0 op_sel_hi:[1,0]\n\tv_pk_mul_f32 %3, %6, %3"
                           : "=&v"(tt[i & 15][0]), "=&v"(tt[i & 15][1]), "+v"(tt[i - 1]), "+v"(tt[i - 2]) : "v"(x[0]), "v"(x[1]), "v"(xm));
            } else if constexpr (i == 16) {
              const f32x2 xm = XP(14);
              asm volatile("v_pk_add_f32 %0, %0, 1.0 op_sel_hi:[1,0]\n\tv_pk_mul_f32 %1, %2, %1" : "+v"(tt[15]), "+v"(tt[14]) : "v"(xm));
            } else {
              const f32x2 xm = XP(15);
              asm volatile("s_nop 0\n\tv_pk_mul_f32 %0, %1, %0" : "+v"(tt[15]) : "v"(xm));
            }
            SB();
            if constexpr (NEXT && i >= 3 && i < 13) {
              constexpr int m = i - 3, ks = m >> 1;
              if constexpr ((m & 1) == 0) {
                if constexpr (ks == 0) N0 = MFMA(A[0], Bw[0][0], f32x16{});
                else if constexpr (ks < 4) N0 = MFMA(A[ks & 3], Bw[ks & 3][0], N0);
                else N0 = MFMA(BF8(a4), BF8(b0), N0);
              } else {
                if constexpr (ks == 0) N1 = MFMA(A[0], Bw[0][1], f32x16{});
                else if constexpr (ks < 4) N1 = MFMA(A[ks & 3], Bw[ks & 3][1], N1);
                else N1 = MFMA(BF8(a4), BF8(b1), N1);
              }
            }
            if constexpr (i == 13) Y0 = MFMA(mk8(sf[0], sf[1]), BF8(M[0][0]), Y0);
            if constexpr (i == 14) Y1 = MFMA(mk8(sf[0], sf[1]), BF8(M[1][0]), Y1);
            SB();
            if constexpr (i >= 1) {
              if constexpr (i >= 2 && i < 17) {
                constexpr int p = i - 2;
                u32 mo;
                asm volatile("v_rcp_f32 %0, %0\n\tv_rcp_f32 %1, %1\n\tv_cvt_pk_bf16_f32 %2, %3, %4"
                             : "+v"(tt[(i - 1) & 15][0]), "+v"(tt[(i - 1) & 15][1]), "=&v"(mo) : "v"(tt[p][0]), "v"(tt[p][1]));
                M[(p >> 2) & 1][(p >> 3) & 1][p & 3] = mo;
              } else if constexpr (i == 1) {
                asm volatile("v_rcp_f32 %0, %0\n\tv_rcp_f32 %1, %1" : "+v"(tt[0][0]), "+v"(tt[0][1]));
              } else {
                u32 mo;
                asm volatile("v_cvt_pk_bf16_f32 %0, %1, %2" : "=v"(mo) : "v"(tt[15][0]), "v"(tt[15][1]));
                M[1][1][3] = mo;
              }
            }
            SB();
          });
          Y0 = MFMA(mk8(sf[2], sf[3]), BF8(M[0][1]), Y0);
          asm volatile("s_nop 0");
          Y1 = MFMA(mk8(sf[2], sf[3]), BF8(M[1][1]), Y1);
          if (NEXT) { sg_cur = sr; a_sk += 128; a_sr += 128; a_ea += 256; PIN(a_sk); PIN(a_sr); PIN(a_ea); }
          e_s += 32; PIN(e_s);
          SB();
          if (NEXT) { if (base1 != base2) { flush(base2); base2 = base1; load_sel(base2); } }
        };
        using Tt = std::integral_constant<bool, true>;
        using Ff = std::integral_constant<bool, false>;
        int t = t0;
        while (t + 2 < t1) { step(Tt{}, XA0, XA1, XB0, XB1, t); step(Tt{}, XB0, XB1, XA0, XA1, t + 1); t += 2; }
        if (t + 1 < t1) { step(Tt{}, XA0, XA1, XB0, XB1, t); step(Ff{}, XB0, XB1, XA0, XA1, t + 1); }
        else step(Ff{}, XA0, XA1, XB0, XB1, t);
        flush(base2);
        asm volatile("s_waitcnt vmcnt(0)" ::: "memory");
      } else {
        load_ap(base); load_sel(base);
        for (int t = t0; t < t1; t++) {
          const int Tb = t * 32;
          const u32x2 ea = *(const u32x2*)(lds + L_EA + (Tb + r) * 8);
          const f32x4 pd = *(const f32x4*)(lds + L_PD + (sg_cur >> 16) * 16);
          u32x4 sk; u32 sr; sg_read(a_sk, a_sr, sk, sr);
          asm volatile("s_waitcnt vmcnt(0)" ::: "memory");
          bf16x8 A[4];
#pragma unroll
          for (int s = 0; s < 4; s++) A[s] = ldl<bf16x8>(aoff[s]);
          const f32x4 ps = ldl<f32x4>(a_ps);
          asm volatile("s_waitcnt lgkmcnt(0)" ::: "memory");
          issue(sk, sr);
          const u32 sg = sg_cur;
          const u32 d2 = geom(sg, ps, pd);
          sg_cur = sr; a_sk += 128; a_sr += 128;
          const int g = (int)(sg >> 16);
          const int gf = __builtin_amdgcn_readlane(g, 0), gl = __builtin_amdgcn_readlane(g, 31);
          if (gl >= base + 8) { flush(base); base = gf; load_ap(base); load_sel(base); }
          f32x16 X0 = {}, X1 = {};
#pragma unroll
          for (int s = 0; s < 4; s++) { X0 = MFMA(A[s], Bw[s][0], X0); X1 = MFMA(A[s], Bw[s][1], X1); }
          for (int wb = base;; wb += 8) {
            if (wb != base) load_ap(wb);
            const u32x4 a4 = make_a4(sg, d2, ea[0], ea[1], wb, wb == base);
            u32x4 b0, b1;
            b0[0] = ap[0][0]; b0[1] = ap[0][1]; b0[2] = xw[0][0]; b0[3] = xw[0][1];
            b1[0] = ap[1][0]; b1[1] = ap[1][1]; b1[2] = xw[1][0]; b1[3] = xw[1][1];
            X0 = MFMA(BF8(a4), BF8(b0), X0);
            X1 = MFMA(BF8(a4), BF8(b1), X1);
            if (gl < wb + 8) { if (wb != base) load_ap(base); break; }
          }
          u32x4 M[2][2];
#pragma unroll
          for (int s = 0; s < 2; s++)
#pragma unroll
            for (int j = 0; j < 4; j++) {
              M[0][s][j] = silu2(X0[8 * s + 2 * j], X0[8 * s + 2 * j + 1]);
              M[1][s][j] = silu2(X1[8 * s + 2 * j], X1[8 * s + 2 * j + 1]);
            }
          for (;;) {
            s16x4 sf[4];
            const u32 d0 = (u32)(Tb + 4 * hh + tq) - slo;
#pragma unroll
            for (int u = 0; u < 4; u++) sf[u] = trl(((d0 + 8u * u) < slen ? a_vr : a_z) + 64 * u);
#pragma unroll
            for (int s = 0; s < 2; s++) {
              const bf16x8 S = mk8(sf[2 * s], sf[2 * s + 1]);
              Y0 = MFMA(S, BF8(M[0][s]), Y0);
              Y1 = MFMA(S, BF8(M[1][s]), Y1);
            }
            if (gl < base + 8) break;
            flush(base); base += 8; load_ap(base); load_sel(base);
          }
        }
        flush(base);
        asm volatile("s_waitcnt vmcnt(0)" ::: "memory");
      }
    }
    STAMP(1, 7);
    if (lane == 0) misc[16 + wv] = (u32)head;
    BAR();
    {
      float* hb = (float*)(lds + L_HB) + wv * MS;
      const int hprev = wv > 0 ? (int)misc[16 + wv - 1] : 127, hnext = wv < 7 ? (int)misc[16 + wv + 1] : 127;
      const bool uniq = (hprev != head) && (hnext != head);
      if (head != 127) {
        const float v = hb[lane];
        hb[lane] = 0.f;
        if (uniq) macc[head * MS + lane] += v;
        else atomicAdd(&macc[head * MS + lane], v);
      }
      if (tid < 144) hist[tid] = 0;
    }
  };

  load_records(0, 4 * MAXR);
  STAMP(1, 2);
  int Pmax = 0, ntot = 0;
  {
    for (int i = tid; i < (99 + 8) * MS / 4; i += 512) *(f32x4*)(macc + 4 * i) = f32x4{0.f, 0.f, 0.f, 0.f};
    if (tid < 106) *(f32x4*)(pacc + 4 * tid) = f32x4{0.f, 0.f, 0.f, 0.f};
    if (tid < 64) *(u32*)(lds + L_ZERO + tid * 4) = 0;
    if (tid < 128) { f32x4 v; v[0] = bf2f(pd0); v[1] = bf2f(pd1); v[2] = bf2f(pd2); v[3] = 0.f; *(f32x4*)(lds + L_PD + tid * 16) = v; }
    if (tid < 144) hist[tid] = 0;
    if (lane == 0) { misc[wv] = (u32)P; misc[8 + wv] = (u32)csum; }
    u32x4 o;
#pragma unroll
    for (int i = 0; i < 4; i++) o[i] = pk_bf16(bflo(wB[i]) * NLOG2E, bfhi(wB[i]) * NLOG2E);
    const u32x4 z4 = {0, 0, 0, 0};
    *(u32x4*)(lds + L_WE + tid * 16) = wA;
    *(u32x4*)(lds + L_WE + 8192 + tid * 16) = o;
    if (tid < 40) *(u32x4*)(lds + L_WE + 16384 + tid * 16) = wC;
    *(u32x4*)(lds + L_H2 + tid * 16) = tid < nn * 8 ? hA : z4;
    *(u32x4*)(lds + L_H2 + 8192 + tid * 16) = tid + 512 < nn * 8 ? hB : z4;
    *(u32x4*)(lds + L_WH + tid * 16) = uA;
    *(u32x4*)(lds + L_WH + 8192 + tid * 16) = uB;
    BAR();
    STAMP(1, 3);
#pragma unroll
    for (int i = 0; i < 8; i++) { Pmax = max(Pmax, (int)misc[i]); ntot += (int)misc[8 + i]; }
    Pmax = __builtin_amdgcn_readfirstlane(Pmax); ntot = __builtin_amdgcn_readfirstlane(ntot);
#pragma unroll
    for (int s = 0; s < 4; s++)
#pragma unroll
      for (int ct = 0; ct < 2; ct++) {
        const u32 p = lds0 + (u32)(L_WE + (64 + 32 * hh + 8 * s + tq) * 128 + (32 * ct + 16 * blk + 4 * tp) * 2);
        Bw[s][ct] = mk8(trl(p), trl(p + 4 * 128));
      }
#pragma unroll
    for (int ct = 0; ct < 2; ct++) {
      const u16* wt = (const u16*)(lds + L_WE + 16384);
      const int col = 32 * ct + r;
      const float w0 = bf2f(wt[col]) * NLOG2E, w1 = bf2f(wt[64 + col]) * NLOG2E, w2 = bf2f(wt[128 + col]) * NLOG2E,
                  w3 = bf2f(wt[192 + col]) * NLOG2E, w4 = bf2f(wt[256 + col]) * NLOG2E;
      const u32 hi = pk_bf16(w0, 0.f) & 0xffffu;
      const u32 lo = pk_bf16(w0 - bf2f(hi), 0.f) & 0xffffu;
      xw[ct][0] = hh ? hi : pk_bf16(w1, w2);
      xw[ct][1] = hh ? lo : pk_bf16(w3, w4);
    }
    {
      const int nt = wv >> 1, ct = wv & 1;
      f32x16 acc = {};
#pragma unroll
      for (int s = 0; s < 4; s++) {
        const u32 p = lds0 + (u32)(L_WE + (32 * hh + 8 * s + tq) * 128 + (32 * ct + 16 * blk + 4 * tp) * 2);
        bf16x8 aw = mk8(trl(p), trl(p + 4 * 128));
        bf16x8 bh = *(const bf16x8*)(lds + L_H2 + (32 * nt + r) * 128 + (4 * hh + s) * 16);
        acc = MFMA(aw, bh, acc);
      }
      const int node = 32 * nt + r;
      char* ap = lds + (node < 106 ? L_AP + node * 128 : L_DESC + 2048 + wv * 128) + (32 * ct + 4 * hh) * 2;
#pragma unroll
      for (int q = 0; q < 4; q++) {
        u32x2 v;
        v[0] = pk_bf16(acc[4 * q] * NLOG2E, acc[4 * q + 1] * NLOG2E);
        v[1] = pk_bf16(acc[4 * q + 2] * NLOG2E, acc[4 * q + 3] * NLOG2E);
        *(u32x2*)(ap + 16 * q) = v;
      }
    }
  }
  if (Pmax <= 4 * MAXR && ntot <= CAP) {
    process(std::integral_constant<bool, true>{});
  } else {
    for (int plo = 0; plo < Pmax; plo += FPPB) {
      BAR();
      load_records(plo, FPPB);
      process(std::integral_constant<bool, false>{});
    }
  }
  STAMP(1, 8);

  BAR();
  {
    int ln = lane;
    asm volatile("" : "+v"(ln));
    const int r = ln & 31, hh = ln >> 5;
    const int G4 = ln >> 4, i16 = ln & 15, tq = i16 >> 2, tp = i16 & 3, blk = G4 & 1;
    const int nt = wv >> 1, ct = wv & 1;
    const int node = 32 * nt + r;
    const int nrow = node < NB ? node : MDUMMY;
    f32x16 acc = {};
#pragma unroll
    for (int s = 0; s < 8; s++) {
      const int kb = (s < 4) ? (32 * hh + 8 * s) : (64 + 32 * hh + 8 * (s - 4));
      const u32 p = lds0 + (u32)(L_WH + (kb + tq) * 128 + (32 * ct + 16 * blk + 4 * tp) * 2);
      const bf16x8 aw = mk8(trl(p), trl(p + 4 * 128));
      bf16x8 bb;
      if (s < 4) {
        bb = *(const bf16x8*)(lds + L_H2 + node * 128 + (4 * hh + s) * 16);
      } else {
        const float* mp = macc + nrow * MS + 32 * hh + 8 * (s - 4);
        const f32x4 m0 = *(const f32x4*)mp, m1 = *(const f32x4*)(mp + 4);
        u32x4 pk;
        pk[0] = pk_bf16(m0[0] * NLN2, m0[1] * NLN2); pk[1] = pk_bf16(m0[2] * NLN2, m0[3] * NLN2);
        pk[2] = pk_bf16(m1[0] * NLN2, m1[1] * NLN2); pk[3] = pk_bf16(m1[2] * NLN2, m1[3] * NLN2);
        bb = __builtin_bit_cast(bf16x8, pk);
      }
      acc = MFMA(aw, bb, acc);
    }
#pragma unroll
    for (int q = 0; q < 4; q++) {
      float u[4];
#pragma unroll
      for (int i = 0; i < 4; i++) {
        const float x = bf2f(pk_bf16(acc[4 * q + i], 0.f) & 0xffffu);
        u[i] = x * __builtin_amdgcn_rcpf(1.0f + __builtin_amdgcn_exp2f(x * NLOG2E));
      }
      u32x2 v; v[0] = pk_bf16(u[0], u[1]); v[1] = pk_bf16(u[2], u[3]);
      *(u32x2*)(lds + L_UT + node * 144 + (32 * ct + 8 * q + 4 * hh) * 2) = v;
    }
  }
  BAR();
#pragma unroll
  for (int k = 0; k < 2; k++) {
    const int i = tid + 512 * k;
    if (i < nn * 8) {
      const int row = i >> 3, chn = i & 7;
      const u32x4 u = *(const u32x4*)(lds + L_UT + row * 144 + chn * 16);
      const u32x4 hv = *(const u32x4*)(lds + L_H2 + i * 16);
      u32x4 o;
#pragma unroll
      for (int j = 0; j < 4; j++) o[j] = pk_bf16(bflo(hv[j]) + bflo(u[j]), bfhi(hv[j]) + bfhi(u[j]));
      *(u32x4*)(out + (size_t)(node0 + row) * 64 + chn * 8) = o;
    }
  }
  if (tid < nn * 3) {
    const int nd = tid / 3, c = tid - nd * 3;
    const float pv = *(const float*)(lds + L_PD + nd * 16 + c * 4);
    const float dlt = bf2f(pk_bf16(pacc[nd * 4 + c], 0.f) & 0xffffu);
    out[(size_t)N * 64 + (size_t)node0 * 3 + tid] = (u16)(pk_bf16(pv + dlt, 0.f) & 0xffffu);
  }
  STAMP(1, 9);
  STAMP_OUT(1, R);
}

extern "C" void kernel_launch(void* const* d_in, const int* in_sizes, int n_in, void* d_out, int out_size, void* d_ws,
                              size_t ws_size, hipStream_t stream) {
  const u16* h = (const u16*)d_in[0];
  const u16* pos = (const u16*)d_in[1];
  const u16* ea = (const u16*)d_in[2];
  const int* src = (const int*)d_in[3];
  const int* dst = (const int*)d_in[4];
  const u16* we = (const u16*)d_in[5];
  const u16* wx = (const u16*)d_in[6];
  const u16* wh = (const u16*)d_in[7];
  const int N = in_sizes[0] / 64, E = in_sizes[3];
  char* ws = (char*)d_ws;
  u32* R = (u32*)ws;
  f32x4* P4 = (f32x4*)(ws + (size_t)10 * 1048576);
  u32* T = (u32*)(ws + (size_t)11 * 1048576);
  (void)hipFuncSetAttribute((const void*)k1_partition, hipFuncAttributeMaxDynamicSharedMemorySize, K1_LDS);
  (void)hipFuncSetAttribute((const void*)k2_main, hipFuncAttributeMaxDynamicSharedMemorySize, K2_LDS);
  k1_partition<<<(E + CH - 1) / CH, 1024, K1_LDS, stream>>>(pos, (const u32x4*)ea, (const i32x4*)src, (const i32x4*)dst, R, P4, T, N, E);
  k2_main<<<(N + NB - 1) / NB, 512, K2_LDS, stream>>>(h, pos, we, wx, wh, R, P4, T, (u16*)d_out, N);
}
```

```cpp
#include <hip/hip_runtime.h>
#include <stdint.h>
#include <type_traits>

typedef unsigned short u16;
typedef unsigned char u8;
typedef unsigned int u32;
typedef short s16x4 __attribute__((ext_vector_type(4)));
typedef __bf16 bf16x2 __attribute__((ext_vector_type(2)));
typedef __bf16 bf16x8 __attribute__((ext_vector_type(8)));
typedef float f32x2 __attribute__((ext_vector_type(2)));
typedef float f32x4 __attribute__((ext_vector_type(4)));
typedef float f32x16 __attribute__((ext_vector_type(16)));
typedef u32 u32x2 __attribute__((ext_vector_type(2)));
typedef u32 u32x3 __attribute__((ext_vector_type(3)));
typedef u32 u32x4 __attribute__((ext_vector_type(4)));
typedef int i32x4 __attribute__((ext_vector_type(4)));
#define LAS __attribute__((address_space(3)))
#define DI __device__ __forceinline__
#define AI __attribute__((always_inline))

constexpr int NB    = 98;
constexpr int CH    = 3128;
constexpr int NCH   = 256;
constexpr int CAP   = 3584;
constexpr int MAXR  = 12;
constexpr int FPPB  = 28;
#define SPLIT 74
constexpr float NLOG2E = -1.4426950408889634f;
constexpr float NLN2   = -0.6931471805599453f;

DI float bf2f(u32 bits16) { return __uint_as_float(bits16 << 16); }
DI float bflo(u32 v) { return __uint_as_float(v << 16); }
DI float bfhi(u32 v) { return __uint_as_float(v & 0xffff0000u); }
DI u32 pk_bf16(float lo, float hi) {
  f32x2 v; v[0] = lo; v[1] = hi;
  return __builtin_bit_cast(u32, __builtin_convertvector(v, bf16x2));
}
template <int CTRL, int RMASK> DI int dpp_i(int x) { return __builtin_amdgcn_update_dpp(0, x, CTRL, RMASK, 0xf, false); }
template <int CTRL> DI float dpp_f(float x) {
  return __builtin_bit_cast(float, __builtin_amdgcn_update_dpp(0, __builtin_bit_cast(int, x), CTRL, 0xf, 0xf, true));
}
DI int wave_incl_scan(int x) {
  x += dpp_i<0x111, 0xf>(x); x += dpp_i<0x112, 0xf>(x); x += dpp_i<0x114, 0xf>(x); x += dpp_i<0x118, 0xf>(x);
  x += dpp_i<0x142, 0xa>(x); x += dpp_i<0x143, 0xc>(x);
  return x;
}
DI float half_reduce(float x) {
  x += dpp_f<0x111>(x); x += dpp_f<0x112>(x); x += dpp_f<0x114>(x); x += dpp_f<0x118>(x);
  x += dpp_f<0x142>(x);
  return x;
}
DI void glds16(u32 lds_addr, u32 voff, const void* base) {
  asm volatile("s_mov_b32 m0, %0\n\ts_nop 0\n\tglobal_load_lds_dwordx4 %1, %2" ::"s"(lds_addr), "v"(voff), "s"(base) : "memory");
}
DI void glds16_lo32(u32 lds_addr, u32 voff, const void* base) {
  asm volatile("s_mov_b32 m0, %0\n\ts_mov_b64 exec, 0xffffffff\n\tglobal_load_lds_dwordx4 %1, %2\n\ts_mov_b64 exec, -1"
               ::"s"(lds_addr), "v"(voff), "s"(base) : "memory");
}
DI void glds_idx(u32 lds_addr, u32 idx, u32 mul, u32 add, const void* base) {
  u32 t;
  asm volatile("s_mov_b32 m0, %1\n\tv_mad_u32_u16 %0, %2, %3, %4\n\tglobal_load_lds_dwordx4 %0, %5"
               : "=&v"(t) : "s"(lds_addr), "v"(idx), "s"(mul), "v"(add), "s"(base) : "memory");
}
DI void glds_idx_lo32(u32 lds_addr, u32 idx, const void* base) {
  u32 t;
  asm volatile("s_mov_b32 m0, %1\n\tv_mad_u32_u16 %0, %2, 16, 0\n\ts_mov_b64 exec, 0xffffffff\n\tglobal_load_lds_dwordx4 %0, %3\n\ts_mov_b64 exec, -1"
               : "=&v"(t) : "s"(lds_addr), "v"(idx), "s"(base) : "memory");
}
DI u32 mad16(u32 a_lo16, u32 b, u32 c) {
  u32 d;
  asm("v_mad_u32_u16 %0, %1, %2, %3" : "=v"(d) : "v"(a_lo16), "s"(b), "v"(c));
  return d;
}
template <class T> DI T ldl(u32 a) { return *(const LAS T*)(uintptr_t)a; }
template <class T> DI void stl(u32 a, T v) { *(LAS T*)(uintptr_t)a = v; }
DI s16x4 trl(u32 a) { return __builtin_amdgcn_ds_read_tr16_b64_v4i16((LAS s16x4*)(uintptr_t)a); }
DI bf16x8 mk8(s16x4 lo, s16x4 hi) {
  return __builtin_bit_cast(bf16x8, __builtin_shufflevector(lo, hi, 0, 1, 2, 3, 4, 5, 6, 7));
}
#define BAR() do { asm volatile("s_waitcnt lgkmcnt(0)" ::: "memory"); __builtin_amdgcn_s_barrier(); asm volatile("" ::: "memory"); } while (0)
#define SB() __builtin_amdgcn_sched_barrier(0)
#define PIN(x) asm volatile("" : "+v"(x))
#define STAMP(k,i)
#define STAMP_DECL
#define STAMP_OUT(k,base)
template <int I, int N, class F> DI void static_for(F&& f) {
  if constexpr (I < N) { f(std::integral_constant<int, I>{}); static_for<I + 1, N>(f); }
}
template <int I> using IC = std::integral_constant<int, I>;
#define MFMA(a, b, c) __builtin_amdgcn_mfma_f32_32x32x16_bf16((a), (b), (c), 0, 0, 0)
#define BF8(x) __builtin_bit_cast(bf16x8, (x))

constexpr int K1_HIST = CH * 12 + 64;
constexpr int K1_OFF = K1_HIST + 1024 + 64;
constexpr int K1_LDS = K1_OFF + 1024 + 64;

__global__ __launch_bounds__(1024) void k1_partition(const u16* __restrict__ pos, const u32x4* __restrict__ ea4,
                                                     const i32x4* __restrict__ src4, const i32x4* __restrict__ dst4,
                                                     u32* __restrict__ R, f32x4* __restrict__ P4, u32* __restrict__ T,
                                                     int N, int E) {
  extern __shared__ __attribute__((aligned(16))) char lds[];
  const int tid = threadIdx.x, lane = tid & 63;
  const int wv = __builtin_amdgcn_readfirstlane(tid >> 6);
  const int w = blockIdx.x;
  u32* hist = (u32*)(lds + K1_HIST);
  u32* off = (u32*)(lds + K1_OFF);
  STAMP_DECL
  STAMP(0, 0);
  int cnt = E - w * CH; cnt = cnt > CH ? CH : cnt; cnt = cnt < 4 ? 4 : cnt;
  const bool act = 4 * tid < cnt;
  const int q = (w * CH >> 2) + (act ? tid : 0);
  const i32x4 d4 = __builtin_nontemporal_load(&dst4[q]);
  const i32x4 s4 = __builtin_nontemporal_load(&src4[q]);
  const u32x4 a0 = __builtin_nontemporal_load(&ea4[2 * q]);
  const u32x4 a1 = __builtin_nontemporal_load(&ea4[2 * q + 1]);
  {
    const int n = w * NB + (tid - 896);
    if (tid >= 896 && tid < 896 + NB && n < N) {
      const u16* p = pos + (size_t)n * 3;
      f32x4 v; v[0] = bf2f(p[0]); v[1] = bf2f(p[1]); v[2] = bf2f(p[2]); v[3] = 0.f;
      P4[n] = v;
    }
  }
  if (tid < 272) hist[tid] = 0;
  BAR();
  STAMP(0, 1);
  int bk[4], gl[4], rk[4];
#pragma unroll
  for (int i = 0; i < 4; i++) {
    const u32 d = (u32)d4[i];
    bk[i] = (int)((d * 42800u) >> 22);
    gl[i] = (int)d - bk[i] * NB;
    bk[i] = act ? bk[i] : 256 + (lane & 15);
    rk[i] = atomicAdd(&hist[bk[i]], 1u);
  }
  STAMP(0, 2);
  BAR();
  STAMP(0, 3);
  {
    u32x4 c = *(const u32x4*)(hist + 4 * lane);
    int s = c[0] + c[1] + c[2] + c[3];
    int inc = wave_incl_scan(s);
    u32x4 o;
    o[0] = inc - s; o[1] = o[0] + c[0]; o[2] = o[1] + c[1]; o[3] = o[2] + c[2];
    *(u32x4*)(off + 4 * lane) = o;
    if (wv == 15) {
#pragma unroll
      for (int i = 0; i < 4; i++) T[(4 * lane + i) * NCH + w] = o[i] | (c[i] << 16);
    }
  }
  STAMP(0, 4);
  if (act) {
#pragma unroll
    for (int i = 0; i < 4; i++) {
      const int slot = (int)(off[bk[i]] + rk[i]);
      u32* p = (u32*)(lds + slot * 12);
      p[0] = i < 2 ? a0[2 * i] : a1[2 * i - 4];
      p[1] = i < 2 ? a0[2 * i + 1] : a1[2 * i - 3];
      p[2] = (u32)s4[i] | ((u32)gl[i] << 16);
    }
  }
  BAR();
  STAMP(0, 5);
  u32x4* Rw = (u32x4*)(R + (size_t)w * CH * 3);
  const int n16 = (cnt * 12 + 15) >> 4;
#pragma unroll
  for (int k = 0; k < 3; k++) {
    const int i = tid + 1024 * k;
    if (i < n16) __builtin_nontemporal_store(*(const u32x4*)(lds + i * 16), &Rw[i]);
  }
  STAMP(0, 6);
  STAMP_OUT(0, R);
}

constexpr int L_GB = 0;
constexpr int L_PB = L_GB + 8 * 4096;
constexpr int L_VB = L_PB + 8 * 512;
constexpr int L_EA = L_VB + 8 * 256;
constexpr int L_SG = L_EA + (CAP + 32) * 8;
constexpr int L_AP = L_SG + (CAP + 96) * 4;
constexpr int MS = 68;
constexpr int MDUMMY = 98;
constexpr int L_MACC = L_AP + 106 * 128;
constexpr int L_HB = L_MACC + 99 * MS * 4;
constexpr int L_PACC = L_HB + 8 * MS * 4;
constexpr int L_HIST = L_PACC + 106 * 16;
constexpr int L_OFF = L_HIST + 144 * 4;
constexpr int L_ZERO = L_OFF + 144 * 4;
constexpr int L_PD = L_ZERO + 256;
constexpr int L_MISC = L_PD + 128 * 16;
constexpr int L_WH = L_MISC + 256;
constexpr int L_H2 = L_WH + 16384;
constexpr int K2_LDS = L_H2 + 16384;
static_assert(K2_LDS <= 163840, "LDS");
static_assert(L_AP % 16 == 0 && L_MACC % 16 == 0 && L_EA % 16 == 0 && L_SG % 16 == 0 && L_HB % 16 == 0 && L_PD % 16 == 0 && L_WH % 16 == 0, "align");
constexpr int L_WE = L_EA;
constexpr int L_DESC = L_GB;
constexpr int L_UT = L_EA;

DI u32 silu2(float z0, float z1) {
  f32x2 z; z[0] = z0; z[1] = z1;
  f32x2 t; t[0] = __builtin_amdgcn_exp2f(z0); t[1] = __builtin_amdgcn_exp2f(z1);
  t = t + 1.0f;
  f32x2 r; r[0] = __builtin_amdgcn_rcpf(t[0]); r[1] = __builtin_amdgcn_rcpf(t[1]);
  f32x2 m = z * r;
  return __builtin_bit_cast(u32, __builtin_convertvector(m, bf16x2));
}

__global__ __launch_bounds__(512) void k2_main(const u16* __restrict__ h, const u16* __restrict__ pos,
                                               const u16* __restrict__ we, const u16* __restrict__ wx,
                                               const u16* __restrict__ wh, const u32* __restrict__ R,
                                               const f32x4* __restrict__ P4, const u32* __restrict__ T,
                                               u16* __restrict__ out, int N) {
  extern __shared__ __attribute__((aligned(16))) char lds[];
  const int tid = threadIdx.x, lane = tid & 63;
  const int wv = __builtin_amdgcn_readfirstlane(tid >> 6);
  const int b = blockIdx.x;
  const int node0 = b * NB;
  int nn = N - node0; nn = nn > NB ? NB : nn; nn = nn < 1 ? 1 : nn;
  const int r = lane & 31, hh = lane >> 5;
  const u32 lds0 = (u32)(uintptr_t)(LAS char*)lds;
  u32* hist = (u32*)(lds + L_HIST);
  u32* off = (u32*)(lds + L_OFF);
  float* macc = (float*)(lds + L_MACC);
  float* pacc = (float*)(lds + L_PACC);
  u32* misc = (u32*)(lds + L_MISC);
  STAMP_DECL
  STAMP(1, 0);

  const u32 tv = T[b * NCH + wv * 32 + r];
  const u32x4* we16 = (const u32x4*)we;
  const u32x4 wA = we16[tid], wB = we16[512 + tid];
  const u32x4 wC = we16[1024 + (tid < 40 ? tid : 0)];
  const u32x4* h16 = (const u32x4*)(h + (size_t)node0 * 64);
  const u32x4 hA = h16[tid < nn * 8 ? tid : 0];
  const u32x4 hB = h16[tid + 512 < nn * 8 ? tid + 512 : 0];
  const u32x4* wh16 = (const u32x4*)wh;
  const u32x4 uA = wh16[tid], uB = wh16[512 + tid];
  const u16* pp = pos + (size_t)(node0 + (tid < nn ? tid : 0)) * 3;
  const u32 pd0 = pp[0], pd1 = pp[1], pd2 = pp[2];
  const float wx0 = bf2f(wx[r]) * NLN2, wx1 = bf2f(wx[32 + r]) * NLN2;

  const u32 cstart = tv & 0xffffu;
  const u32 ccnt = lane < 32 ? (tv >> 16) : 0u;
  const int np = (int)((ccnt + 15) >> 4);
  const int pinc = wave_incl_scan(np);
  const int pexc = pinc - np;
  const int P = __builtin_amdgcn_readlane(pinc, 31);
  const int csum = __builtin_amdgcn_readlane(wave_incl_scan((int)ccnt), 31);
  STAMP(1, 1);

  const int G4 = lane >> 4, i16 = lane & 15, tq = i16 >> 2, tp = i16 & 3, blk = G4 & 1;
  bf16x8 Bw[4][2];
  u32 xw[2][2];
  u32x3 rec[MAXR];
  u32 vmask = 0;
  int nrounds = 0;
#pragma unroll
  for (int rr = 8; rr < MAXR; rr++) rec[rr] = u32x3{0, 0, 0};
  u32* desc = (u32*)(lds + L_DESC + wv * 256);
  auto load_records = [&](int plo, int ppb) AI {
    int k0 = plo - pexc; k0 = k0 < 0 ? 0 : k0;
    int k1 = plo + ppb - pexc; k1 = k1 > np ? np : k1;
    const u32 gbase = (u32)((wv * 32 + r) * CH) + cstart;
    for (int k = k0; k < k1; k++) {
      u32 nrec = ccnt - 16 * k; nrec = nrec > 16 ? 16 : nrec;
      desc[pexc + k - plo] = (gbase + 16 * k) | (nrec << 24);
    }
    int Pb = P - plo; Pb = Pb < 0 ? 0 : Pb; Pb = Pb > ppb ? ppb : Pb;
    nrounds = (Pb + 3) >> 2;
    vmask = 0;
    auto rounds = [&](auto lo_c, auto hi_c) AI {
      constexpr int lo = decltype(lo_c)::value, hi = decltype(hi_c)::value;
      u32 dd[hi - lo];
#pragma unroll
      for (int rr = lo; rr < hi; rr++) dd[rr - lo] = desc[4 * rr + G4];
#pragma unroll
      for (int rr = lo; rr < hi; rr++) {
        const u32 d = (4 * rr + G4 < Pb) ? dd[rr - lo] : 0u;
        const bool v = (u32)i16 < (d >> 24);
        const u32 gi = (d & 0xffffffu) + (v ? i16 : 0);
        rec[rr] = *(const u32x3*)(R + (size_t)gi * 3);
        vmask |= v ? (1u << rr) : 0u;
      }
    };
    rounds(IC<0>{}, IC<8>{});
    if (nrounds > 8) rounds(IC<8>{}, IC<10>{});
    if (nrounds > 10) rounds(IC<10>{}, IC<12>{});
  };
  auto process = [&](auto fast_tag) AI {
    constexpr bool ALLOW_FAST = decltype(fast_tag)::value;
    STAMP(1, 4);
    u32 rk[MAXR];
    auto ranks = [&](auto lo_c, auto hi_c) AI {
      constexpr int lo = decltype(lo_c)::value, hi = decltype(hi_c)::value;
#pragma unroll
      for (int rr = lo; rr < hi; rr++) {
        const u32 g = (vmask >> rr) & 1u ? (rec[rr][2] >> 16) : 128u + (lane & 15);
        rk[rr] = atomicAdd(&hist[g], 1u);
      }
    };
#pragma unroll
    for (int rr = 8; rr < MAXR; rr++) rk[rr] = 0;
    ranks(IC<0>{}, IC<8>{});
    if (nrounds > 8) ranks(IC<8>{}, IC<10>{});
    if (nrounds > 10) ranks(IC<10>{}, IC<12>{});
    BAR();
    STAMP(1, 5);
    int n, glast;
    {
      u32x2 c = *(const u32x2*)(hist + 2 * lane);
      int s = c[0] + c[1];
      int inc = wave_incl_scan(s);
      u32x2 o; o[0] = inc - s; o[1] = o[0] + c[0];
      *(u32x2*)(off + 2 * lane) = o;
      n = __builtin_amdgcn_readlane(inc, 63);
      if (lane < 16) off[128 + lane] = n;
      const unsigned long long m1 = __ballot(c[1] != 0), m0 = __ballot(c[0] != 0);
      const int l1 = m1 ? 63 - __builtin_clzll(m1) : -1, l0 = m0 ? 63 - __builtin_clzll(m0) : -1;
      glast = max(2 * l1 + 1, 2 * l0); glast = glast < 0 ? 0 : glast;
    }
    const int ntiles = (n + 31) >> 5;
    auto scatter = [&](auto lo_c, auto hi_c) AI {
      constexpr int lo = decltype(lo_c)::value, hi = decltype(hi_c)::value;
      u32 ps[hi - lo];
#pragma unroll
      for (int rr = lo; rr < hi; rr++) {
        const bool v = (vmask >> rr) & 1u;
        const u32 g = v ? (rec[rr][2] >> 16) : 128u;
        ps[rr - lo] = off[g] + rk[rr];
      }
#pragma unroll
      for (int rr = lo; rr < hi; rr++) {
        const bool v = (vmask >> rr) & 1u;
        const u32 p = v ? ps[rr - lo] : (u32)(CAP + (lane & 31));
        u32x2 e; e[0] = rec[rr][0]; e[1] = rec[rr][1];
        *(u32x2*)(lds + L_EA + p * 8) = e;
        *(u32*)(lds + L_SG + (v ? p : p + 64u) * 4) = rec[rr][2];
      }
    };
    scatter(IC<0>{}, IC<8>{});
    if (nrounds > 8) scatter(IC<8>{}, IC<10>{});
    if (nrounds > 10) scatter(IC<10>{}, IC<12>{});
    {
      const int ps = n + lane + 64 * (wv & 1);
      if (wv >= 6 && ps < ntiles * 32 + 64) {
        u32x2 z; z[0] = 0; z[1] = 0;
        if (ps < ntiles * 32) *(u32x2*)(lds + L_EA + ps * 8) = z;
        *(u32*)(lds + L_SG + ps * 4) = (u32)glast << 16;
      }
    }
    BAR();
    STAMP(1, 6);

    int ln = lane;
    asm volatile("" : "+v"(ln));
    const int r = ln & 31, hh = ln >> 5;
    const int G4 = ln >> 4, i16 = ln & 15, tq = i16 >> 2, tp = i16 & 3, blk = G4 & 1;
    const int kk = ln >> 3, c8 = ln & 7;
    u32 gcst[4], aoff[4];
#pragma unroll
    for (int i = 0; i < 4; i++) gcst[i] = (u32)((c8 ^ i ^ (((kk >> 1) & 1) << 2)) * 16);
    {
      const int ii = r & 3, k2 = r >> 2;
#pragma unroll
      for (int s = 0; s < 4; s++)
        aoff[s] = lds0 + (u32)(L_GB + wv * 4096 + (ii * 8 + k2) * 128 + (((4 * hh + s) ^ ii ^ (((k2 >> 1) & 1) << 2)) * 16));
    }
    const int slot = (wv & 3) * 2 + (wv >> 2);
    auto cum = [&](int k) { return (ntiles * ((k >> 1) * 128 + (k & 1) * SPLIT)) >> 9; };
    const int t0 = cum(slot), t1 = cum(slot + 1);
    int head = 127;
    if (t0 < t1) {
      const u32 gb = lds0 + L_GB + wv * 4096;
      const u32 pb = lds0 + L_PB + wv * 512;
      const u32 vb = lds0 + L_VB + wv * 256;
      const u32 a_ps = pb + r * 16;
      const u32 a_vw = vb + r * 8;
      const u32 a_vr = vb + (4 * hh + tq) * 8;
      const u32 a_z = lds0 + L_ZERO;
      auto sg_read = [&](u32 a_k, u32 a_r, u32x4& sk, u32& sr) AI {
        sk[0] = ldl<u32>(a_k); sk[1] = ldl<u32>(a_k + 4); sk[2] = ldl<u32>(a_k + 8); sk[3] = ldl<u32>(a_k + 12);
        sr = ldl<u32>(a_r);
      };
      auto issue = [&](const u32x4& sk, u32 sr) AI {
        glds_idx(gb, sk[0], 128u, gcst[0], h);
        glds_idx(gb + 1024, sk[1], 128u, gcst[1], h);
        glds_idx(gb + 2048, sk[2], 128u, gcst[2], h);
        glds_idx(gb + 3072, sk[3], 128u, gcst[3], h);
        glds_idx_lo32(pb, sr, P4);
      };
      u32 a_sk = lds0 + (u32)(L_SG + (t0 * 32 + 4 * kk) * 4), a_sr = lds0 + (u32)(L_SG + (t0 * 32 + r) * 4);
      u32 sg_cur;
      { u32x4 sk; sg_read(a_sk, a_sr, sk, sg_cur); issue(sk, sg_cur); a_sk += 128; a_sr += 128; }
      const int tl = (t0 + ln < t1) ? t0 + ln : t1 - 1;
      const int gfv = (int)(*(const u32*)(lds + L_SG + tl * 128) >> 16);
      const int glv = (int)(*(const u32*)(lds + L_SG + tl * 128 + 124) >> 16);
      const bool wide = __ballot(glv - gfv >= 8) != 0ull || (t1 - t0) > 64;
      int base = __builtin_amdgcn_readlane(gfv, 0);
      if ((int)__builtin_amdgcn_readfirstlane(off[base]) < t0 * 32) head = base;
      const int gspan = __builtin_amdgcn_readfirstlane((int)(*(const u32*)(lds + L_SG + (t1 * 32 - 1) * 4) >> 16));
      f32x16 Y0 = {}, Y1 = {};
      u32 ap[2][2];
      u32 slo, slen;
      auto load_ap = [&](int bs) AI {
#pragma unroll
        for (int ct = 0; ct < 2; ct++) {
          s16x4 v = trl(lds0 + (u32)(L_AP + (bs + 4 * hh + tq) * 128 + (32 * ct + 16 * blk + 4 * tp) * 2));
          u32x2 u = __builtin_bit_cast(u32x2, v);
          ap[ct][0] = u[0]; ap[ct][1] = u[1];
        }
      };
      auto load_sel = [&](int bs) AI {
        const int nl = bs + 4 * blk + tp;
        slo = off[nl]; slen = off[nl + 1] - slo;
      };
      auto flush = [&](int bs) AI {
        u32 ma[4];
        float o0[4], o1[4];
        const u32 mrow = lds0 + (u32)(L_MACC + hh * (MS * 4) + 4 * r) + (u32)(bs * (MS * 4));
        if (bs + 7 <= gspan && (head == 127 || bs > head)) {
#pragma unroll
          for (int q = 0; q < 4; q++) ma[q] = mrow + (u32)(2 * q * (MS * 4));
        } else {
#pragma unroll
          for (int q = 0; q < 4; q++) {
            const int nd = bs + 2 * q + hh;
            u32 a = mrow + (u32)(2 * q * (MS * 4));
            a = nd > gspan ? lds0 + (u32)(L_MACC + MDUMMY * (MS * 4)) + 4 * r : a;
            a = nd == head ? lds0 + (u32)(L_HB + wv * (MS * 4)) + 4 * r : a;
            ma[q] = a;
          }
        }
#pragma unroll
        for (int q = 0; q < 4; q++) { o0[q] = ldl<float>(ma[q]); o1[q] = ldl<float>(ma[q] + 128); }
        SB();
#pragma unroll
        for (int q = 0; q < 4; q++) { stl<float>(ma[q], o0[q] + Y0[4 * q]); stl<float>(ma[q] + 128, o1[q] + Y1[4 * q]); }
        float pr[12];
#pragma unroll
        for (int q = 0; q < 4; q++)
#pragma unroll
          for (int c = 1; c < 4; c++) pr[q * 3 + c - 1] = half_reduce(Y0[4 * q + c] * wx0 + Y1[4 * q + c] * wx1);
        if (r == 31) {
#pragma unroll
          for (int q = 0; q < 4; q++)
#pragma unroll
            for (int c = 0; c < 3; c++) atomicAdd(&pacc[(bs + 2 * q + hh) * 4 + c], pr[q * 3 + c]);
        }
#pragma unroll
        for (int i = 0; i < 16; i++) { Y0[i] = 0.f; Y1[i] = 0.f; }
      };
      auto geom = [&](const f32x4& ps, const f32x4& pd) AI {
        const float dx = pd[0] - ps[0], dy = pd[1] - ps[1], dz = pd[2] - ps[2];
        u32x2 v; v[0] = pk_bf16(1.0f, dx); v[1] = pk_bf16(dy, dz);
        stl<u32x2>(a_vw, v);
        return pk_bf16(dx * dx + dy * dy + dz * dz, 0.f);
      };
      auto make_a4 = [&](u32 sg, u32 d2, u32 ea0, u32 ea1, int wb, bool with_x) AI {
        const u32 t4 = (sg >> 16) - (u32)(wb + 4 * hh);
        unsigned long long oh = 0x3F80ull << ((t4 & 3u) * 16u);
        if (t4 >= 4u) oh = 0ull;
        u32x4 a4; a4[0] = (u32)oh; a4[1] = (u32)(oh >> 32);
        a4[2] = with_x ? (hh ? d2 : ea0) : 0u; a4[3] = with_x ? (hh ? d2 : ea1) : 0u;
        return a4;
      };

      if (ALLOW_FAST && !wide) {
        int base1 = base, base2 = base;
        unsigned long long chg = 0;
        {
          int bb = base;
          for (int i = 1; i < t1 - t0; i++) {
            const int gl1 = __builtin_amdgcn_readlane(glv, i);
            if (gl1 >= bb + 8) { bb = __builtin_amdgcn_readlane(gfv, i); chg |= 1ull << i; }
          }
        }
        load_ap(base1);
        load_sel(base2);
        f32x16 XA0, XA1, XB0, XB1;
        u32 a_ea = lds0 + (u32)(L_EA + (t0 * 32 + r) * 8);
        u32 e_s = (u32)(t0 * 32 + 4 * hh + tq);
        const u32 a_pd = lds0 + L_PD;
        {
          const u32x2 ea = ldl<u32x2>(a_ea);
          const f32x4 pd = ldl<f32x4>(a_pd + (sg_cur >> 16) * 16);
          u32x4 sk; u32 sr; sg_read(a_sk, a_sr, sk, sr);
          asm volatile("s_waitcnt vmcnt(0)" ::: "memory");
          bf16x8 A[4];
#pragma unroll
          for (int s = 0; s < 4; s++) A[s] = ldl<bf16x8>(aoff[s]);
          const f32x4 ps = ldl<f32x4>(a_ps);
          asm volatile("s_waitcnt lgkmcnt(0)" ::: "memory");
          issue(sk, sr);
          const u32 d2 = geom(ps, pd);
          XA0 = f32x16{}; XA1 = f32x16{};
#pragma unroll
          for (int s = 0; s < 4; s++) { XA0 = MFMA(A[s], Bw[s][0], XA0); XA1 = MFMA(A[s], Bw[s][1], XA1); }
          const u32x4 a4 = make_a4(sg_cur, d2, ea[0], ea[1], base1, true);
          u32x4 b0, b1;
          b0[0] = ap[0][0]; b0[1] = ap[0][1]; b0[2] = xw[0][0]; b0[3] = xw[0][1];
          b1[0] = ap[1][0]; b1[1] = ap[1][1]; b1[2] = xw[1][0]; b1[3] = xw[1][1];
          XA0 = MFMA(BF8(a4), BF8(b0), XA0); XA1 = MFMA(BF8(a4), BF8(b1), XA1);
          sg_cur = sr; a_sk += 128; a_sr += 128; a_ea += 256;
          asm volatile("s_nop 7\n\ts_nop 7" : "+v"(XA0), "+v"(XA1));
        }
        u32 a_zv = a_z; PIN(a_zv);
        u32 a_pdv = a_pd; PIN(a_pdv);
        auto step = [&](auto next_tag, f32x16& C0, f32x16& C1, f32x16& N0, f32x16& N1, int t) AI {
          constexpr bool NEXT = decltype(next_tag)::value;
          if (NEXT) {
            if (__builtin_expect((chg >> (t + 1 - t0)) & 1ull, 0)) { base1 = __builtin_amdgcn_readlane(gfv, t + 1 - t0); load_ap(base1); }
          }
          u32x4 M[2][2];
          bf16x8 A[4]; u32x2 ea; u32x4 sk; u32 sr; f32x4 ps, pd;
          s16x4 sf[4];
          u32x4 a4, b0, b1;
          f32x2 tt[16];
          SB();
#define XP(p) (((p) >> 2) & 1 ? __builtin_shufflevector(C1, C1, 8 * ((p) >> 3) + 2 * ((p) & 3), 8 * ((p) >> 3) + 2 * ((p) & 3) + 1) \
                              : __builtin_shufflevector(C0, C0, 8 * ((p) >> 3) + 2 * ((p) & 3), 8 * ((p) >> 3) + 2 * ((p) & 3) + 1))
#define MSET(p, v) M[((p) >> 2) & 1][((p) >> 3) & 1][(p) & 3] = (v)
          static_for<0, 20>([&](auto ic) AI {
            constexpr int k = decltype(ic)::value - 1;
            constexpr bool R = (k - 1 >= 0 && k - 1 < 16), Cv = (k - 3 >= 0 && k - 3 < 16), E = (k + 1 < 16), Ad = (k >= 0 && k < 16), Mu = (k - 2 >= 0 && k - 2 < 16);
            if constexpr (R && Cv && E && Ad && Mu) {
              const f32x2 xe = XP(k + 1), xm = XP(k - 2);
              u32 mo;
              asm volatile("v_rcp_f32 %0, %0\n\tv_rcp_f32 %1, %1\n\tv_cvt_pk_bf16_f32 %2, %7, %8\n\tv_exp_f32 %3, %9\n\tv_exp_f32 %4, %10\n\t"
                           "v_pk_add_f32 %5, %5, 1.0 op_sel_hi:[1,0]\n\tv_pk_mul_f32 %6, %11, %6"
                           : "+v"(tt[k - 1][0]), "+v"(tt[k - 1][1]), "=&v"(mo), "=&v"(tt[k + 1][0]), "=&v"(tt[k + 1][1]), "+v"(tt[k]), "+v"(tt[k - 2])
                           : "v"(tt[k - 3][0]), "v"(tt[k - 3][1]), "v"(xe[0]), "v"(xe[1]), "v"(xm));
              MSET(k - 3, mo);
            } else {
              if constexpr (R) asm volatile("v_rcp_f32 %0, %0\n\tv_rcp_f32 %1, %1" : "+v"(tt[(k - 1) & 15][0]), "+v"(tt[(k - 1) & 15][1]));
              if constexpr (Cv) {
                u32 mo;
                if constexpr (k == 18) asm volatile("v_cvt_pk_bf16_f32 %0, %1, %2\n\ts_nop 1" : "=v"(mo) : "v"(tt[(k - 3) & 15][0]), "v"(tt[(k - 3) & 15][1]));
                else asm volatile("v_cvt_pk_bf16_f32 %0, %1, %2" : "=v"(mo) : "v"(tt[(k - 3) & 15][0]), "v"(tt[(k - 3) & 15][1]));
                MSET((k - 3) & 15, mo);
              }
              if constexpr (E) {
                const f32x2 xe = XP((k + 1) & 15);
                asm volatile("v_exp_f32 %0, %2\n\tv_exp_f32 %1, %3" : "=&v"(tt[(k + 1) & 15][0]), "=&v"(tt[(k + 1) & 15][1]) : "v"(xe[0]), "v"(xe[1]));
              }
              if constexpr (Ad) asm volatile("v_pk_add_f32 %0, %0, 1.0 op_sel_hi:[1,0]" : "+v"(tt[k & 15]));
              if constexpr (Mu) {
                const f32x2 xm = XP((k - 2) & 15);
                asm volatile("v_pk_mul_f32 %0, %1, %0" : "+v"(tt[(k - 2) & 15]) : "v"(xm));
              }
            }
            SB();
            if constexpr (k == -1) {
              if constexpr (NEXT) {
                asm volatile("s_waitcnt vmcnt(0)" ::: "memory");
#pragma unroll
                for (int s = 0; s < 4; s++) A[s] = ldl<bf16x8>(aoff[s]);
                ps = ldl<f32x4>(a_ps);
                pd = ldl<f32x4>(a_pdv + __builtin_amdgcn_ubfe(sg_cur, 16, 7) * 16);
                ea = ldl<u32x2>(a_ea);
                sg_read(a_sk, a_sr, sk, sr);
              }
            }
            if constexpr (k == 0) {
              u32 s0, s1, s2, s3;
              unsigned long long m0_, m1_, m2_, m3_;
              asm volatile("v_sub_u32 %0, %8, %9\n\tv_add_u32 %1, 8, %0\n\tv_add_u32 %2, 16, %0\n\tv_add_u32 %3, 24, %0\n\t"
                           "v_cmp_lt_u32 %4, %0, %10\n\tv_cmp_lt_u32 %5, %1, %10\n\tv_cmp_lt_u32 %6, %2, %10\n\tv_cmp_lt_u32 %7, %3, %10\n\t"
                           "v_cndmask_b32 %0, %11, %12, %4\n\tv_cndmask_b32 %1, %11, %12, %5\n\tv_cndmask_b32 %2, %11, %12, %6\n\tv_cndmask_b32 %3, %11, %12, %7"
                           : "=&v"(s0), "=&v"(s1), "=&v"(s2), "=&v"(s3), "=&s"(m0_), "=&s"(m1_), "=&s"(m2_), "=&s"(m3_)
                           : "v"(e_s), "v"(slo), "v"(slen), "v"(a_zv), "v"(a_vr));
              sf[0] = trl(s0); sf[1] = trl(s1 + 64); sf[2] = trl(s2 + 128); sf[3] = trl(s3 + 192);
            }
            if constexpr (k == 1 && NEXT) {
              asm volatile("s_waitcnt lgkmcnt(0)" ::: "memory");
              issue(sk, sr);
            }
            if constexpr (NEXT && k >= 2 && k < 12) {
              constexpr int m = k - 2, ks = m >> 1;
              if constexpr (k == 4) {
                const u32 d2 = geom(ps, pd);
                a4 = make_a4(sg_cur, d2, ea[0], ea[1], base1, true);
                b0[0] = ap[0][0]; b0[1] = ap[0][1]; b0[2] = xw[0][0]; b0[3] = xw[0][1];
                b1[0] = ap[1][0]; b1[1] = ap[1][1]; b1[2] = xw[1][0]; b1[3] = xw[1][1];
                SB();
              }
              if constexpr ((m & 1) == 0) {
                if constexpr (ks == 0) N0 = MFMA(A[0], Bw[0][0], f32x16{});
                else if constexpr (ks < 4) N0 = MFMA(A[ks & 3], Bw[ks & 3][0], N0);
                else N0 = MFMA(BF8(a4), BF8(b0), N0);
              } else {
                if constexpr (ks == 0) N1 = MFMA(A[0], Bw[0][1], f32x16{});
                else if constexpr (ks < 4) N1 = MFMA(A[ks & 3], Bw[ks & 3][1], N1);
                else N1 = MFMA(BF8(a4), BF8(b1), N1);
              }
            }
            if constexpr (k == 12) Y0 = MFMA(mk8(sf[0], sf[1]), BF8(M[0][0]), Y0);
            if constexpr (k == 13) Y1 = MFMA(mk8(sf[0], sf[1]), BF8(M[1][0]), Y1);
            if constexpr (k == 15) {
              if (NEXT) { sg_cur = sr; a_sk += 128; a_sr += 128; a_ea += 256; PIN(a_sk); PIN(a_sr); PIN(a_ea); }
              e_s += 32; PIN(e_s);
            }
            if constexpr (k == 16) Y0 = MFMA(mk8(sf[2], sf[3]), BF8(M[0][1]), Y0);
            SB();
          });
          Y1 = MFMA(mk8(sf[2], sf[3]), BF8(M[1][1]), Y1);
          SB();
          if (NEXT) { if (__builtin_expect(base1 != base2, 0)) { flush(base2); base2 = base1; load_sel(base2); } }
        };
        using Tt = std::integral_constant<bool, true>;
        using Ff = std::integral_constant<bool, false>;
        int t = t0;
        while (t + 2 < t1) { step(Tt{}, XA0, XA1, XB0, XB1, t); step(Tt{}, XB0, XB1, XA0, XA1, t + 1); t += 2; }
        if (t + 1 < t1) { step(Tt{}, XA0, XA1, XB0, XB1, t); step(Ff{}, XB0, XB1, XA0, XA1, t + 1); }
        else step(Ff{}, XA0, XA1, XB0, XB1, t);
        flush(base2);
        asm volatile("s_waitcnt vmcnt(0)" ::: "memory");
      } else {
        load_ap(base); load_sel(base);
        for (int t = t0; t < t1; t++) {
          const int Tb = t * 32;
          const u32x2 ea = *(const u32x2*)(lds + L_EA + (Tb + r) * 8);
          const f32x4 pd = *(const f32x4*)(lds + L_PD + (sg_cur >> 16) * 16);
          u32x4 sk; u32 sr; sg_read(a_sk, a_sr, sk, sr);
          asm volatile("s_waitcnt vmcnt(0)" ::: "memory");
          bf16x8 A[4];
#pragma unroll
          for (int s = 0; s < 4; s++) A[s] = ldl<bf16x8>(aoff[s]);
          const f32x4 ps = ldl<f32x4>(a_ps);
          asm volatile("s_waitcnt lgkmcnt(0)" ::: "memory");
          issue(sk, sr);
          const u32 sg = sg_cur;
          const u32 d2 = geom(ps, pd);
          sg_cur = sr; a_sk += 128; a_sr += 128;
          const int g = (int)(sg >> 16);
          const int gf = __builtin_amdgcn_readlane(g, 0), gl = __builtin_amdgcn_readlane(g, 31);
          if (gl >= base + 8) { flush(base); base = gf; load_ap(base); load_sel(base); }
          f32x16 X0 = {}, X1 = {};
#pragma unroll
          for (int s = 0; s < 4; s++) { X0 = MFMA(A[s], Bw[s][0], X0); X1 = MFMA(A[s], Bw[s][1], X1); }
          for (int wb = base;; wb += 8) {
            if (wb != base) load_ap(wb);
            const u32x4 a4 = make_a4(sg, d2, ea[0], ea[1], wb, wb == base);
            u32x4 b0, b1;
            b0[0] = ap[0][0]; b0[1] = ap[0][1]; b0[2] = xw[0][0]; b0[3] = xw[0][1];
            b1[0] = ap[1][0]; b1[1] = ap[1][1]; b1[2] = xw[1][0]; b1[3] = xw[1][1];
            X0 = MFMA(BF8(a4), BF8(b0), X0);
            X1 = MFMA(BF8(a4), BF8(b1), X1);
            if (gl < wb + 8) { if (wb != base) load_ap(base); break; }
          }
          u32x4 M[2][2];
#pragma unroll
          for (int s = 0; s < 2; s++)
#pragma unroll
            for (int j = 0; j < 4; j++) {
              M[0][s][j] = silu2(X0[8 * s + 2 * j], X0[8 * s + 2 * j + 1]);
              M[1][s][j] = silu2(X1[8 * s + 2 * j], X1[8 * s + 2 * j + 1]);
            }
          for (;;) {
            s16x4 sf[4];
            const u32 d0 = (u32)(Tb + 4 * hh + tq) - slo;
#pragma unroll
            for (int u = 0; u < 4; u++) sf[u] = trl(((d0 + 8u * u) < slen ? a_vr : a_z) + 64 * u);
#pragma unroll
            for (int s = 0; s < 2; s++) {
              const bf16x8 S = mk8(sf[2 * s], sf[2 * s + 1]);
              Y0 = MFMA(S, BF8(M[0][s]), Y0);
              Y1 = MFMA(S, BF8(M[1][s]), Y1);
            }
            if (gl < base + 8) break;
            flush(base); base += 8; load_ap(base); load_sel(base);
          }
        }
        flush(base);
        asm volatile("s_waitcnt vmcnt(0)" ::: "memory");
      }
    }
    STAMP(1, 7);
    if (lane == 0) misc[16 + slot] = (u32)head;
    BAR();
    {
      float* hb = (float*)(lds + L_HB) + wv * MS;
      const int hprev = slot > 0 ? (int)misc[16 + slot - 1] : 127, hnext = slot < 7 ? (int)misc[16 + slot + 1] : 127;
      const bool uniq = (hprev != head) && (hnext != head);
      if (head != 127) {
        const float v = hb[lane];
        hb[lane] = 0.f;
        if (uniq) macc[head * MS + lane] += v;
        else atomicAdd(&macc[head * MS + lane], v);
      }
      if (tid < 144) hist[tid] = 0;
    }
  };

  load_records(0, 4 * MAXR);
  STAMP(1, 2);
  int Pmax = 0, ntot = 0;
  {
    for (int i = tid; i < (99 + 8) * MS / 4; i += 512) *(f32x4*)(macc + 4 * i) = f32x4{0.f, 0.f, 0.f, 0.f};
    if (tid < 106) *(f32x4*)(pacc + 4 * tid) = f32x4{0.f, 0.f, 0.f, 0.f};
    if (tid < 64) *(u32*)(lds + L_ZERO + tid * 4) = 0;
    if (tid < 128) { f32x4 v; v[0] = bf2f(pd0); v[1] = bf2f(pd1); v[2] = bf2f(pd2); v[3] = 0.f; *(f32x4*)(lds + L_PD + tid * 16) = v; }
    if (tid < 144) hist[tid] = 0;
    if (lane == 0) { misc[wv] = (u32)P; misc[8 + wv] = (u32)csum; }
    u32x4 o;
#pragma unroll
    for (int i = 0; i < 4; i++) o[i] = pk_bf16(bflo(wB[i]) * NLOG2E, bfhi(wB[i]) * NLOG2E);
    const u32x4 z4 = {0, 0, 0, 0};
    *(u32x4*)(lds + L_WE + tid * 16) = wA;
    *(u32x4*)(lds + L_WE + 8192 + tid * 16) = o;
    if (tid < 40) *(u32x4*)(lds + L_WE + 16384 + tid * 16) = wC;
    *(u32x4*)(lds + L_H2 + tid * 16) = tid < nn * 8 ? hA : z4;
    *(u32x4*)(lds + L_H2 + 8192 + tid * 16) = tid + 512 < nn * 8 ? hB : z4;
    *(u32x4*)(lds + L_WH + tid * 16) = uA;
    *(u32x4*)(lds + L_WH + 8192 + tid * 16) = uB;
    BAR();
    STAMP(1, 3);
#pragma unroll
    for (int i = 0; i < 8; i++) { Pmax = max(Pmax, (int)misc[i]); ntot += (int)misc[8 + i]; }
    Pmax = __builtin_amdgcn_readfirstlane(Pmax); ntot = __builtin_amdgcn_readfirstlane(ntot);
#pragma unroll
    for (int s = 0; s < 4; s++)
#pragma unroll
      for (int ct = 0; ct < 2; ct++) {
        const u32 p = lds0 + (u32)(L_WE + (64 + 32 * hh + 8 * s + tq) * 128 + (32 * ct + 16 * blk + 4 * tp) * 2);
        Bw[s][ct] = mk8(trl(p), trl(p + 4 * 128));
      }
#pragma unroll
    for (int ct = 0; ct < 2; ct++) {
      const u16* wt = (const u16*)(lds + L_WE + 16384);
      const int col = 32 * ct + r;
      const float w0 = bf2f(wt[col]) * NLOG2E, w1 = bf2f(wt[64 + col]) * NLOG2E, w2 = bf2f(wt[128 + col]) * NLOG2E,
                  w3 = bf2f(wt[192 + col]) * NLOG2E, w4 = bf2f(wt[256 + col]) * NLOG2E;
      const u32 hi = pk_bf16(w0, 0.f) & 0xffffu;
      const u32 lo = pk_bf16(w0 - bf2f(hi), 0.f) & 0xffffu;
      xw[ct][0] = hh ? hi : pk_bf16(w1, w2);
      xw[ct][1] = hh ? lo : pk_bf16(w3, w4);
    }
    {
      const int nt = wv >> 1, ct = wv & 1;
      f32x16 acc = {};
#pragma unroll
      for (int s = 0; s < 4; s++) {
        const u32 p = lds0 + (u32)(L_WE + (32 * hh + 8 * s + tq) * 128 + (32 * ct + 16 * blk + 4 * tp) * 2);
        bf16x8 aw = mk8(trl(p), trl(p + 4 * 128));
        bf16x8 bh = *(const bf16x8*)(lds + L_H2 + (32 * nt + r) * 128 + (4 * hh + s) * 16);
        acc = MFMA(aw, bh, acc);
      }
      const int node = 32 * nt + r;
      char* ap = lds + (node < 106 ? L_AP + node * 128 : L_DESC + 2048 + wv * 128) + (32 * ct + 4 * hh) * 2;
#pragma unroll
      for (int q = 0; q < 4; q++) {
        u32x2 v;
        v[0] = pk_bf16(acc[4 * q] * NLOG2E, acc[4 * q + 1] * NLOG2E);
        v[1] = pk_bf16(acc[4 * q + 2] * NLOG2E, acc[4 * q + 3] * NLOG2E);
        *(u32x2*)(ap + 16 * q) = v;
      }
    }
  }
  if (Pmax <= 4 * MAXR && ntot <= CAP) {
    process(std::integral_constant<bool, true>{});
  } else {
    for (int plo = 0; plo < Pmax; plo += FPPB) {
      BAR();
      load_records(plo, FPPB);
      process(std::integral_constant<bool, false>{});
    }
  }
  STAMP(1, 8);

  BAR();
  {
    int ln = lane;
    asm volatile("" : "+v"(ln));
    const int r = ln & 31, hh = ln >> 5;
    const int G4 = ln >> 4, i16 = ln & 15, tq = i16 >> 2, tp = i16 & 3, blk = G4 & 1;
    const int nt = wv >> 1, ct = wv & 1;
    const int node = 32 * nt + r;
    const int nrow = node < NB ? node : MDUMMY;
    f32x16 acc = {};
#pragma unroll
    for (int s = 0; s < 8; s++) {
      const int kb = (s < 4) ? (32 * hh + 8 * s) : (64 + 32 * hh + 8 * (s - 4));
      const u32 p = lds0 + (u32)(L_WH + (kb + tq) * 128 + (32 * ct + 16 * blk + 4 * tp) * 2);
      const bf16x8 aw = mk8(trl(p), trl(p + 4 * 128));
      bf16x8 bb;
      if (s < 4) {
        bb = *(const bf16x8*)(lds + L_H2 + node * 128 + (4 * hh + s) * 16);
      } else {
        const float* mp = macc + nrow * MS + 32 * hh + 8 * (s - 4);
        const f32x4 m0 = *(const f32x4*)mp, m1 = *(const f32x4*)(mp + 4);
        u32x4 pk;
        pk[0] = pk_bf16(m0[0] * NLN2, m0[1] * NLN2); pk[1] = pk_bf16(m0[2] * NLN2, m0[3] * NLN2);
        pk[2] = pk_bf16(m1[0] * NLN2, m1[1] * NLN2); pk[3] = pk_bf16(m1[2] * NLN2, m1[3] * NLN2);
        bb = __builtin_bit_cast(bf16x8, pk);
      }
      acc = MFMA(aw, bb, acc);
    }
#pragma unroll
    for (int q = 0; q < 4; q++) {
      float u[4];
#pragma unroll
      for (int i = 0; i < 4; i++) {
        const float x = bf2f(pk_bf16(acc[4 * q + i], 0.f) & 0xffffu);
        u[i] = x * __builtin_amdgcn_rcpf(1.0f + __builtin_amdgcn_exp2f(x * NLOG2E));
      }
      u32x2 v; v[0] = pk_bf16(u[0], u[1]); v[1] = pk_bf16(u[2], u[3]);
      *(u32x2*)(lds + L_UT + node * 144 + (32 * ct + 8 * q + 4 * hh) * 2) = v;
    }
  }
  BAR();
#pragma unroll
  for (int k = 0; k < 2; k++) {
    const int i = tid + 512 * k;
    if (i < nn * 8) {
      const int row = i >> 3, chn = i & 7;
      const u32x4 u = *(const u32x4*)(lds + L_UT + row * 144 + chn * 16);
      const u32x4 hv = *(const u32x4*)(lds + L_H2 + i * 16);
      u32x4 o;
#pragma unroll
      for (int j = 0; j < 4; j++) o[j] = pk_bf16(bflo(hv[j]) + bflo(u[j]), bfhi(hv[j]) + bfhi(u[j]));
      *(u32x4*)(out + (size_t)(node0 + row) * 64 + chn * 8) = o;
    }
  }
  if (tid < nn * 3) {
    const int nd = tid / 3, c = tid - nd * 3;
    const float pv = *(const float*)(lds + L_PD + nd * 16 + c * 4);
    const float dlt = bf2f(pk_bf16(pacc[nd * 4 + c], 0.f) & 0xffffu);
    out[(size_t)N * 64 + (size_t)node0 * 3 + tid] = (u16)(pk_bf16(pv + dlt, 0.f) & 0xffffu);
  }
  STAMP(1, 9);
  STAMP_OUT(1, R);
}

extern "C" void kernel_launch(void* const* d_in, const int* in_sizes, int n_in, void* d_out, int out_size, void* d_ws,
                              size_t ws_size, hipStream_t stream) {
  const u16* h = (const u16*)d_in[0];
  const u16* pos = (const u16*)d_in[1];
  const u16* ea = (const u16*)d_in[2];
  const int* src = (const int*)d_in[3];
  const int* dst = (const int*)d_in[4];
  const u16* we = (const u16*)d_in[5];
  const u16* wx = (const u16*)d_in[6];
  const u16* wh = (const u16*)d_in[7];
  const int N = in_sizes[0] / 64, E = in_sizes[3];
  char* ws = (char*)d_ws;
  u32* R = (u32*)ws;
  f32x4* P4 = (f32x4*)(ws + (size_t)10 * 1048576);
  u32* T = (u32*)(ws + (size_t)11 * 1048576);
  (void)hipFuncSetAttribute((const void*)k1_partition, hipFuncAttributeMaxDynamicSharedMemorySize, K1_LDS);
  (void)hipFuncSetAttribute((const void*)k2_main, hipFuncAttributeMaxDynamicSharedMemorySize, K2_LDS);
  k1_partition<<<(E + CH - 1) / CH, 1024, K1_LDS, stream>>>(pos, (const u32x4*)ea, (const i32x4*)src, (const i32x4*)dst, R, P4, T, N, E);
  k2_main<<<(N + NB - 1) / NB, 512, K2_LDS, stream>>>(h, pos, we, wx, wh, R, P4, T, (u16*)d_out, N);
}
```

```cpp
#include <hip/hip_runtime.h>
#include <stdint.h>
#include <type_traits>

typedef unsigned short u16;
typedef unsigned char u8;
typedef unsigned int u32;
typedef short s16x4 __attribute__((ext_vector_type(4)));
typedef __bf16 bf16x2 __attribute__((ext_vector_type(2)));
typedef __bf16 bf16x8 __attribute__((ext_vector_type(8)));
typedef float f32x2 __attribute__((ext_vector_type(2)));
typedef float f32x4 __attribute__((ext_vector_type(4)));
typedef float f32x16 __attribute__((ext_vector_type(16)));
typedef u32 u32x2 __attribute__((ext_vector_type(2)));
typedef u32 u32x3 __attribute__((ext_vector_type(3)));
typedef u32 u32x4 __attribute__((ext_vector_type(4)));
typedef int i32x4 __attribute__((ext_vector_type(4)));
#define LAS __attribute__((address_space(3)))
#define DI __device__ __forceinline__
#define AI __attribute__((always_inline))

constexpr int NB    = 98;
constexpr int CH    = 3128;
constexpr int NCH   = 256;
constexpr int CAP   = 3392;
constexpr int MAXR  = 12;
constexpr int FPPB  = 26;
#define SPLIT 74
constexpr float NLOG2E = -1.4426950408889634f;
constexpr float NLN2   = -0.6931471805599453f;

DI float bf2f(u32 bits16) { return __uint_as_float(bits16 << 16); }
DI float bflo(u32 v) { return __uint_as_float(v << 16); }
DI float bfhi(u32 v) { return __uint_as_float(v & 0xffff0000u); }
DI u32 pk_bf16(float lo, float hi) {
  f32x2 v; v[0] = lo; v[1] = hi;
  return __builtin_bit_cast(u32, __builtin_convertvector(v, bf16x2));
}
template <int CTRL, int RMASK> DI int dpp_i(int x) { return __builtin_amdgcn_update_dpp(0, x, CTRL, RMASK, 0xf, false); }
template <int CTRL> DI float dpp_f(float x) {
  return __builtin_bit_cast(float, __builtin_amdgcn_update_dpp(0, __builtin_bit_cast(int, x), CTRL, 0xf, 0xf, true));
}
DI int wave_incl_scan(int x) {
  x += dpp_i<0x111, 0xf>(x); x += dpp_i<0x112, 0xf>(x); x += dpp_i<0x114, 0xf>(x); x += dpp_i<0x118, 0xf>(x);
  x += dpp_i<0x142, 0xa>(x); x += dpp_i<0x143, 0xc>(x);
  return x;
}
DI float half_reduce(float x) {
  x += dpp_f<0x111>(x); x += dpp_f<0x112>(x); x += dpp_f<0x114>(x); x += dpp_f<0x118>(x);
  x += dpp_f<0x142>(x);
  return x;
}
DI void glds16(u32 lds_addr, u32 voff, const void* base) {
  asm volatile("s_mov_b32 m0, %0\n\ts_nop 0\n\tglobal_load_lds_dwordx4 %1, %2" ::"s"(lds_addr), "v"(voff), "s"(base) : "memory");
}
DI void glds16_lo32(u32 lds_addr, u32 voff, const void* base) {
  asm volatile("s_mov_b32 m0, %0\n\ts_mov_b64 exec, 0xffffffff\n\tglobal_load_lds_dwordx4 %1, %2\n\ts_mov_b64 exec, -1"
               ::"s"(lds_addr), "v"(voff), "s"(base) : "memory");
}
DI void glds_idx(u32 lds_addr, u32 idx, u32 mul, u32 add, const void* base) {
  u32 t;
  asm volatile("s_mov_b32 m0, %1\n\tv_mad_u32_u16 %0, %2, %3, %4\n\tglobal_load_lds_dwordx4 %0, %5"
               : "=&v"(t) : "s"(lds_addr), "v"(idx), "s"(mul), "v"(add), "s"(base) : "memory");
}
DI void glds_idx_lo32(u32 lds_addr, u32 idx, const void* base) {
  u32 t;
  asm volatile("s_mov_b32 m0, %1\n\tv_mad_u32_u16 %0, %2, 16, 0\n\ts_mov_b64 exec, 0xffffffff\n\tglobal_load_lds_dwordx4 %0, %3\n\ts_mov_b64 exec, -1"
               : "=&v"(t) : "s"(lds_addr), "v"(idx), "s"(base) : "memory");
}
DI u32 mad16(u32 a_lo16, u32 b, u32 c) {
  u32 d;
  asm("v_mad_u32_u16 %0, %1, %2, %3" : "=v"(d) : "v"(a_lo16), "s"(b), "v"(c));
  return d;
}
template <int HALF> DI void half_write_b64(u32& prev, u32 a, u32x2 v) {
  if constexpr (HALF == 0)
    asm volatile("s_mov_b64 exec, 0xffffffff\n\tds_write_b64 %1, %2\n\tv_mov_b32 %0, %1\n\ts_mov_b64 exec, -1" : "+v"(prev) : "v"(a), "v"(v) : "memory");
  else
    asm volatile("s_mov_b64 exec, %3\n\tds_write_b64 %1, %2\n\tv_mov_b32 %0, %1\n\ts_mov_b64 exec, -1" : "+v"(prev) : "v"(a), "v"(v), "s"(0xffffffff00000000ull) : "memory");
}
template <class T> DI T ldl(u32 a) { return *(const LAS T*)(uintptr_t)a; }
template <class T> DI void stl(u32 a, T v) { *(LAS T*)(uintptr_t)a = v; }
DI s16x4 trl(u32 a) { return __builtin_amdgcn_ds_read_tr16_b64_v4i16((LAS s16x4*)(uintptr_t)a); }
DI bf16x8 mk8(s16x4 lo, s16x4 hi) {
  return __builtin_bit_cast(bf16x8, __builtin_shufflevector(lo, hi, 0, 1, 2, 3, 4, 5, 6, 7));
}
#define BAR() do { asm volatile("s_waitcnt lgkmcnt(0)" ::: "memory"); __builtin_amdgcn_s_barrier(); asm volatile("" ::: "memory"); } while (0)
#define SB() __builtin_amdgcn_sched_barrier(0)
#define PIN(x) asm volatile("" : "+v"(x))
#define STAMP(k,i)
#define STAMP_DECL
#define STAMP_OUT(k,base)
template <int I, int N, class F> DI void static_for(F&& f) {
  if constexpr (I < N) { f(std::integral_constant<int, I>{}); static_for<I + 1, N>(f); }
}
template <int I> using IC = std::integral_constant<int, I>;
#define MFMA(a, b, c) __builtin_amdgcn_mfma_f32_32x32x16_bf16((a), (b), (c), 0, 0, 0)
#define BF8(x) __builtin_bit_cast(bf16x8, (x))

constexpr int K1_HIST = CH * 12 + 64;
constexpr int K1_OFF = K1_HIST + 1024 + 64;
constexpr int K1_LDS = K1_OFF + 1024 + 64;

__global__ __launch_bounds__(1024) void k1_partition(const u16* __restrict__ pos, const u32x4* __restrict__ ea4,
                                                     const i32x4* __restrict__ src4, const i32x4* __restrict__ dst4,
                                                     u32* __restrict__ R, f32x4* __restrict__ P4, u32* __restrict__ T,
                                                     int N, int E) {
  extern __shared__ __attribute__((aligned(16))) char lds[];
  const int tid = threadIdx.x, lane = tid & 63;
  const int wv = __builtin_amdgcn_readfirstlane(tid >> 6);
  const int w = blockIdx.x;
  u32* hist = (u32*)(lds + K1_HIST);
  u32* off = (u32*)(lds + K1_OFF);
  STAMP_DECL
  STAMP(0, 0);
  int cnt = E - w * CH; cnt = cnt > CH ? CH : cnt; cnt = cnt < 4 ? 4 : cnt;
  const bool act = 4 * tid < cnt;
  const int q = (w * CH >> 2) + (act ? tid : 0);
  const i32x4 d4 = __builtin_nontemporal_load(&dst4[q]);
  const i32x4 s4 = __builtin_nontemporal_load(&src4[q]);
  const u32x4 a0 = __builtin_nontemporal_load(&ea4[2 * q]);
  const u32x4 a1 = __builtin_nontemporal_load(&ea4[2 * q + 1]);
  {
    const int n = w * NB + (tid - 896);
    if (tid >= 896 && tid < 896 + NB && n < N) {
      const u16* p = pos + (size_t)n * 3;
      f32x4 v; v[0] = bf2f(p[0]); v[1] = bf2f(p[1]); v[2] = bf2f(p[2]); v[3] = 0.f;
      P4[n] = v;
    }
  }
  if (tid < 272) hist[tid] = 0;
  BAR();
  STAMP(0, 1);
  int bk[4], gl[4], rk[4];
#pragma unroll
  for (int i = 0; i < 4; i++) {
    const u32 d = (u32)d4[i];
    bk[i] = (int)((d * 42800u) >> 22);
    gl[i] = (int)d - bk[i] * NB;
    bk[i] = act ? bk[i] : 256 + (lane & 15);
    rk[i] = atomicAdd(&hist[bk[i]], 1u);
  }
  STAMP(0, 2);
  BAR();
  STAMP(0, 3);
  {
    u32x4 c = *(const u32x4*)(hist + 4 * lane);
    int s = c[0] + c[1] + c[2] + c[3];
    int inc = wave_incl_scan(s);
    u32x4 o;
    o[0] = inc - s; o[1] = o[0] + c[0]; o[2] = o[1] + c[1]; o[3] = o[2] + c[2];
    *(u32x4*)(off + 4 * lane) = o;
    if (wv == 15) {
#pragma unroll
      for (int i = 0; i < 4; i++) T[(4 * lane + i) * NCH + w] = o[i] | (c[i] << 16);
    }
  }
  STAMP(0, 4);
  if (act) {
#pragma unroll
    for (int i = 0; i < 4; i++) {
      const int slot = (int)(off[bk[i]] + rk[i]);
      u32* p = (u32*)(lds + slot * 12);
      p[0] = i < 2 ? a0[2 * i] : a1[2 * i - 4];
      p[1] = i < 2 ? a0[2 * i + 1] : a1[2 * i - 3];
      p[2] = (u32)s4[i] | ((u32)gl[i] << 16);
    }
  }
  BAR();
  STAMP(0, 5);
  u32x4* Rw = (u32x4*)(R + (size_t)w * CH * 3);
  const int n16 = (cnt * 12 + 15) >> 4;
#pragma unroll
  for (int k = 0; k < 3; k++) {
    const int i = tid + 1024 * k;
    if (i < n16) __builtin_nontemporal_store(*(const u32x4*)(lds + i * 16), &Rw[i]);
  }
  STAMP(0, 6);
  STAMP_OUT(0, R);
}

constexpr int L_GB = 0;
constexpr int L_PB = L_GB + 8 * 4096;
constexpr int L_D2 = L_PB + 8 * 1024;
constexpr int L_ST = L_D2 + 8 * 512;
constexpr int L_EA = L_ST + 8 * 2048;
constexpr int L_SG = L_EA + (CAP + 32) * 8;
constexpr int SGPAD = 128;
constexpr int L_AP = L_SG + (CAP + 160) * 4;
constexpr int MS = 68;
constexpr int MDUMMY = 98;
constexpr int L_MACC = L_AP + 105 * 128;
constexpr int L_HB = L_MACC + 99 * MS * 4;
constexpr int L_PACC = L_HB + 8 * MS * 4;
constexpr int L_HIST = L_PACC + 105 * 16;
constexpr int L_OFF = L_HIST + 144 * 4;
constexpr int L_ZERO = L_OFF + 144 * 4;
constexpr int L_LUT = L_ZERO + 256;
constexpr int L_PD = L_LUT + 64;
constexpr int L_MISC = L_PD + 128 * 16;
constexpr int L_H2 = L_MISC + 256;
constexpr int K2_LDS = L_H2 + 12800;
static_assert(K2_LDS <= 163840, "LDS");
static_assert(L_AP % 16 == 0 && L_MACC % 16 == 0 && L_EA % 16 == 0 && L_SG % 16 == 0 && L_HB % 16 == 0 && L_PD % 16 == 0 && L_H2 % 16 == 0, "align");
constexpr int L_WE = L_EA;
constexpr int L_DESC = L_GB;
constexpr int L_UT = L_EA;
constexpr int L_WH = L_EA + 128 * 144;

DI u32 silu2(float z0, float z1) {
  f32x2 z; z[0] = z0; z[1] = z1;
  f32x2 t; t[0] = __builtin_amdgcn_exp2f(z0); t[1] = __builtin_amdgcn_exp2f(z1);
  t = t + 1.0f;
  f32x2 r; r[0] = __builtin_amdgcn_rcpf(t[0]); r[1] = __builtin_amdgcn_rcpf(t[1]);
  f32x2 m = z * r;
  return __builtin_bit_cast(u32, __builtin_convertvector(m, bf16x2));
}

__global__ __launch_bounds__(512) void k2_main(const u16* __restrict__ h, const u16* __restrict__ pos,
                                               const u16* __restrict__ we, const u16* __restrict__ wx,
                                               const u16* __restrict__ wh, const u32* __restrict__ R,
                                               const f32x4* __restrict__ P4, const u32* __restrict__ T,
                                               u16* __restrict__ out, int N) {
  extern __shared__ __attribute__((aligned(16))) char lds[];
  const int tid = threadIdx.x, lane = tid & 63;
  const int wv = __builtin_amdgcn_readfirstlane(tid >> 6);
  const int b = blockIdx.x;
  const int node0 = b * NB;
  int nn = N - node0; nn = nn > NB ? NB : nn; nn = nn < 1 ? 1 : nn;
  const int r = lane & 31, hh = lane >> 5;
  const u32 lds0 = (u32)(uintptr_t)(LAS char*)lds;
  u32* hist = (u32*)(lds + L_HIST);
  u32* off = (u32*)(lds + L_OFF);
  float* macc = (float*)(lds + L_MACC);
  float* pacc = (float*)(lds + L_PACC);
  u32* misc = (u32*)(lds + L_MISC);
  STAMP_DECL
  STAMP(1, 0);

  const u32 tv = T[b * NCH + wv * 32 + r];
  const u32x4* we16 = (const u32x4*)we;
  const u32x4 wA = we16[tid], wB = we16[512 + tid];
  const u32x4 wC = we16[1024 + (tid < 40 ? tid : 0)];
  const u32x4* h16 = (const u32x4*)(h + (size_t)node0 * 64);
  const u32x4 hA = h16[tid < nn * 8 ? tid : 0];
  const u32x4 hB = h16[tid + 512 < nn * 8 ? tid + 512 : 0];
  const u32x4* wh16 = (const u32x4*)wh;
  const u16* pp = pos + (size_t)(node0 + (tid < nn ? tid : 0)) * 3;
  const u32 pd0 = pp[0], pd1 = pp[1], pd2 = pp[2];
  const float wx0 = bf2f(wx[r]) * NLN2, wx1 = bf2f(wx[32 + r]) * NLN2;

  const u32 cstart = tv & 0xffffu;
  const u32 ccnt = lane < 32 ? (tv >> 16) : 0u;
  const int np = (int)((ccnt + 15) >> 4);
  const int pinc = wave_incl_scan(np);
  const int pexc = pinc - np;
  const int P = __builtin_amdgcn_readlane(pinc, 31);
  const int csum = __builtin_amdgcn_readlane(wave_incl_scan((int)ccnt), 31);
  STAMP(1, 1);

  const int G4 = lane >> 4, i16 = lane & 15, tq = i16 >> 2, tp = i16 & 3, blk = G4 & 1;
  bf16x8 Bw[4][2];
  u32 xw[2][2];
  u32x3 rec[MAXR];
  u32 vmask = 0;
  int nrounds = 0;
#pragma unroll
  for (int rr = 8; rr < MAXR; rr++) rec[rr] = u32x3{0, 0, 0};
  u32* desc = (u32*)(lds + L_DESC + wv * 256);
  auto load_records = [&](int plo, int ppb) AI {
    int k0 = plo - pexc; k0 = k0 < 0 ? 0 : k0;
    int k1 = plo + ppb - pexc; k1 = k1 > np ? np : k1;
    const u32 gbase = (u32)((wv * 32 + r) * CH) + cstart;
    for (int k = k0; k < k1; k++) {
      u32 nrec = ccnt - 16 * k; nrec = nrec > 16 ? 16 : nrec;
      desc[pexc + k - plo] = (gbase + 16 * k) | (nrec << 24);
    }
    int Pb = P - plo; Pb = Pb < 0 ? 0 : Pb; Pb = Pb > ppb ? ppb : Pb;
    nrounds = (Pb + 3) >> 2;
    vmask = 0;
    auto rounds = [&](auto lo_c, auto hi_c) AI {
      constexpr int lo = decltype(lo_c)::value, hi = decltype(hi_c)::value;
      u32 dd[hi - lo];
#pragma unroll
      for (int rr = lo; rr < hi; rr++) dd[rr - lo] = desc[4 * rr + G4];
#pragma unroll
      for (int rr = lo; rr < hi; rr++) {
        const u32 d = (4 * rr + G4 < Pb) ? dd[rr - lo] : 0u;
        const bool v = (u32)i16 < (d >> 24);
        const u32 gi = (d & 0xffffffu) + (v ? i16 : 0);
        rec[rr] = *(const u32x3*)(R + (size_t)gi * 3);
        vmask |= v ? (1u << rr) : 0u;
      }
    };
    rounds(IC<0>{}, IC<8>{});
    if (nrounds > 8) rounds(IC<8>{}, IC<10>{});
    if (nrounds > 10) rounds(IC<10>{}, IC<12>{});
  };
  auto process = [&](auto fast_tag) AI {
    constexpr bool ALLOW_FAST = decltype(fast_tag)::value;
    STAMP(1, 4);
    u32 rk[MAXR];
    auto ranks = [&](auto lo_c, auto hi_c) AI {
      constexpr int lo = decltype(lo_c)::value, hi = decltype(hi_c)::value;
#pragma unroll
      for (int rr = lo; rr < hi; rr++) {
        const u32 g = (vmask >> rr) & 1u ? (rec[rr][2] >> 16) : 128u + (lane & 15);
        rk[rr] = atomicAdd(&hist[g], 1u);
      }
    };
#pragma unroll
    for (int rr = 8; rr < MAXR; rr++) rk[rr] = 0;
    ranks(IC<0>{}, IC<8>{});
    if (nrounds > 8) ranks(IC<8>{}, IC<10>{});
    if (nrounds > 10) ranks(IC<10>{}, IC<12>{});
    BAR();
    STAMP(1, 5);
    int n, glast;
    {
      u32x2 c = *(const u32x2*)(hist + 2 * lane);
      int s = c[0] + c[1];
      int inc = wave_incl_scan(s);
      u32x2 o; o[0] = inc - s; o[1] = o[0] + c[0];
      *(u32x2*)(off + 2 * lane) = o;
      n = __builtin_amdgcn_readlane(inc, 63);
      if (lane < 16) off[128 + lane] = n;
      const unsigned long long m1 = __ballot(c[1] != 0), m0 = __ballot(c[0] != 0);
      const int l1 = m1 ? 63 - __builtin_clzll(m1) : -1, l0 = m0 ? 63 - __builtin_clzll(m0) : -1;
      glast = max(2 * l1 + 1, 2 * l0); glast = glast < 0 ? 0 : glast;
    }
    const int ntiles = (n + 31) >> 5;
    auto scatter = [&](auto lo_c, auto hi_c) AI {
      constexpr int lo = decltype(lo_c)::value, hi = decltype(hi_c)::value;
      u32 ps[hi - lo];
#pragma unroll
      for (int rr = lo; rr < hi; rr++) {
        const bool v = (vmask >> rr) & 1u;
        const u32 g = v ? (rec[rr][2] >> 16) : 128u;
        ps[rr - lo] = off[g] + rk[rr];
      }
#pragma unroll
      for (int rr = lo; rr < hi; rr++) {
        const bool v = (vmask >> rr) & 1u;
        const u32 p = v ? ps[rr - lo] : (u32)(CAP + (lane & 31));
        u32x2 e; e[0] = rec[rr][0]; e[1] = rec[rr][1];
        *(u32x2*)(lds + L_EA + p * 8) = e;
        *(u32*)(lds + L_SG + (v ? p : p + (u32)SGPAD) * 4) = rec[rr][2];
      }
    };
    scatter(IC<0>{}, IC<8>{});
    if (nrounds > 8) scatter(IC<8>{}, IC<10>{});
    if (nrounds > 10) scatter(IC<10>{}, IC<12>{});
    {
      const int ps = n + lane + 64 * (wv & 3);
      if (wv >= 4 && ps < ntiles * 32 + SGPAD) {
        u32x2 z; z[0] = 0; z[1] = 0;
        if (ps < ntiles * 32) *(u32x2*)(lds + L_EA + ps * 8) = z;
        *(u32*)(lds + L_SG + ps * 4) = 127u << 16;
      }
    }
    BAR();
    STAMP(1, 6);

    int ln = lane;
    asm volatile("" : "+v"(ln));
    const int r = ln & 31, hh = ln >> 5;
    const int G4 = ln >> 4, i16 = ln & 15, tq = i16 >> 2, tp = i16 & 3, blk = G4 & 1;
    const int kk = ln >> 3, c8 = ln & 7;
    u32 gcst[4], aoff[4];
#pragma unroll
    for (int i = 0; i < 4; i++) gcst[i] = (u32)((c8 ^ i ^ (((kk >> 1) & 1) << 2)) * 16);
    {
      const int ii = r & 3, k2 = r >> 2;
#pragma unroll
      for (int s = 0; s < 4; s++)
        aoff[s] = lds0 + (u32)(L_GB + wv * 4096 + (ii * 8 + k2) * 128 + (((4 * hh + s) ^ ii ^ (((k2 >> 1) & 1) << 2)) * 16));
    }
    const int slot = (wv & 3) * 2 + (wv >> 2);
    auto cum = [&](int k) { return (ntiles * ((k >> 1) * 128 + (k & 1) * SPLIT)) >> 9; };
    const int t0 = cum(slot), t1 = cum(slot + 1);
    int head = 127;
    if (t0 < t1) {
      const u32 gb = lds0 + L_GB + wv * 4096;
      const u32 pb = lds0 + L_PB + wv * 1024;
      const u32 d2b = lds0 + L_D2 + wv * 512;
      const u32 a_vw = d2b + r * 8;
      const u32 a_vr = d2b + (4 * hh + tq) * 8;
      const u32 a_z = lds0 + L_ZERO;
      auto sk_read = [&](u32 a_k, u32x4& sk) AI {
        sk[0] = ldl<u32>(a_k); sk[1] = ldl<u32>(a_k + 4); sk[2] = ldl<u32>(a_k + 8); sk[3] = ldl<u32>(a_k + 12);
      };
      auto issue_h = [&](const u32x4& sk) AI {
        glds_idx(gb, sk[0], 128u, gcst[0], h);
        glds_idx(gb + 1024, sk[1], 128u, gcst[1], h);
        glds_idx(gb + 2048, sk[2], 128u, gcst[2], h);
        glds_idx(gb + 3072, sk[3], 128u, gcst[3], h);
      };
      u32 a_sk = lds0 + (u32)(L_SG + (t0 * 32 + 4 * kk) * 4), a_sr = lds0 + (u32)(L_SG + (t0 * 32 + r) * 4);
      { u32x4 sk; sk_read(a_sk, sk); issue_h(sk); a_sk += 128; }
      const int tl = (t0 + ln < t1) ? t0 + ln : t1 - 1;
      const int gfv = (int)(*(const u32*)(lds + L_SG + tl * 128) >> 16);
      const int glv = min((int)(*(const u32*)(lds + L_SG + tl * 128 + 124) >> 16), glast);
      const bool wide = __ballot(glv - gfv >= 8) != 0ull || (t1 - t0) > 64;
      int base = __builtin_amdgcn_readlane(gfv, 0);
      if ((int)__builtin_amdgcn_readfirstlane(off[base]) < t0 * 32) head = base;
      const int gspan = __builtin_amdgcn_readlane(glv, 63);
      f32x16 Y0 = {}, Y1 = {};
      u32 ap[2][2];
      auto load_ap = [&](int bs) AI {
#pragma unroll
        for (int ct = 0; ct < 2; ct++) {
          s16x4 v = trl(lds0 + (u32)(L_AP + (bs + 4 * hh + tq) * 128 + (32 * ct + 16 * blk + 4 * tp) * 2));
          u32x2 u = __builtin_bit_cast(u32x2, v);
          ap[ct][0] = u[0]; ap[ct][1] = u[1];
        }
      };
      auto flush = [&](int bs) AI {
        u32 ma[4];
        float o0[4], o1[4];
        const u32 mrow = lds0 + (u32)(L_MACC + hh * (MS * 4) + 4 * r) + (u32)(bs * (MS * 4));
        if (bs + 7 <= gspan && (head == 127 || bs > head)) {
#pragma unroll
          for (int q = 0; q < 4; q++) ma[q] = mrow + (u32)(2 * q * (MS * 4));
        } else {
#pragma unroll
          for (int q = 0; q < 4; q++) {
            const int nd = bs + 2 * q + hh;
            u32 a = mrow + (u32)(2 * q * (MS * 4));
            a = nd > gspan ? lds0 + (u32)(L_MACC + MDUMMY * (MS * 4)) + 4 * r : a;
            a = nd == head ? lds0 + (u32)(L_HB + wv * (MS * 4)) + 4 * r : a;
            ma[q] = a;
          }
        }
#pragma unroll
        for (int q = 0; q < 4; q++) { o0[q] = ldl<float>(ma[q]); o1[q] = ldl<float>(ma[q] + 128); }
        float pr[12];
#pragma unroll
        for (int q = 0; q < 4; q++)
#pragma unroll
          for (int c = 1; c < 4; c++) pr[q * 3 + c - 1] = Y0[4 * q + c] * wx0 + Y1[4 * q + c] * wx1;
        SB();
#pragma unroll
        for (int q = 0; q < 4; q++) { stl<float>(ma[q], o0[q] + Y0[4 * q]); stl<float>(ma[q] + 128, o1[q] + Y1[4 * q]); }
#pragma unroll
        for (int j = 0; j < 12; j++) pr[j] = half_reduce(pr[j]);
        if (r == 31) {
#pragma unroll
          for (int q = 0; q < 4; q++)
#pragma unroll
            for (int c = 0; c < 3; c++) atomicAdd(&pacc[(bs + 2 * q + hh) * 4 + c], pr[q * 3 + c]);
        }
#pragma unroll
        for (int i = 0; i < 16; i++) { Y0[i] = 0.f; Y1[i] = 0.f; }
      };

      if (ALLOW_FAST && !wide) {
        int base1 = base, base2 = base;
        unsigned long long chg = 0;
        {
          int bb = base;
          for (int i = 1; i < t1 - t0; i++) {
            const int gl1 = __builtin_amdgcn_readlane(glv, i);
            if (gl1 >= bb + 8) { bb = __builtin_amdgcn_readlane(gfv, i); chg |= 1ull << i; }
          }
        }
        load_ap(base1);
        u32 bh = (u32)(base1 + 4 * hh);
        f32x16 XA0, XA1, XB0, XB1;
        const u32 a_ps = pb + ln * 16;
        const u32 a_d2w = d2b + ln * 8;
        const u32 st_row = lds0 + (u32)(L_ST + wv * 2048 + r * 64);
        const u32 a_st = lds0 + (u32)(L_ST + wv * 2048 + (4 * hh + tq) * 64 + (4 * blk + tp) * 8);
        u32 a_pd = lds0 + L_PD, a_lut = lds0 + L_LUT;
        PIN(a_pd); PIN(a_lut);
        u32 a_sp = lds0 + (u32)(L_SG + (t0 * 32 + ln) * 4);
        u32 a_x = hh ? d2b + r * 8 : lds0 + (u32)(L_EA + (t0 * 32 + r) * 8);
        const u32 incB = hh ? (u32)-256 : 256u;
        u32 st_prev = st_row, st_g;
        u32x2 vv;
        u32 sgp;
        auto geom = [&](u32 sg, const f32x4& ps, const f32x4& pd) AI {
          const float dx = pd[0] - ps[0], dy = pd[1] - ps[1], dz = pd[2] - ps[2];
          const bool pad = sg >= (127u << 16);
          vv[0] = pad ? 0u : pk_bf16(1.0f, dx); vv[1] = pad ? 0u : pk_bf16(dy, dz);
          u32x2 d; d[0] = pk_bf16(dx * dx + dy * dy + dz * dz, 0.f); d[1] = d[0];
          stl<u32x2>(a_d2w, d);
          st_g = st_row + (sg >> 16) * 8;
        };
        auto st_write = [&](auto half_c, int bs) AI {
          constexpr int HALF = decltype(half_c)::value;
          u32 a = min(st_g - (u32)(bs * 8), st_row + 56u);
          half_write_b64<HALF>(st_prev, a, vv);
        };
        u32 sg_cur;
        {
          sgp = ldl<u32>(a_sp); a_sp += 256;
          glds_idx(pb, sgp, 16u, 0u, P4);
          const u32 sg0 = ldl<u32>(a_sr); a_sr += 128;
          const f32x4 pd = ldl<f32x4>(a_pd + __builtin_amdgcn_ubfe(sgp, 16, 7) * 16);
          const u32 sgn = ldl<u32>(a_sp); a_sp += 256;
          u32x4 sk; sk_read(a_sk, sk); a_sk += 128;
          sg_cur = ldl<u32>(a_sr); a_sr += 128;
          asm volatile("s_waitcnt vmcnt(0)" ::: "memory");
          bf16x8 A[4];
#pragma unroll
          for (int s = 0; s < 4; s++) A[s] = ldl<bf16x8>(aoff[s]);
          const f32x4 ps = ldl<f32x4>(a_ps);
          asm volatile("s_waitcnt lgkmcnt(0)" ::: "memory");
          issue_h(sk);
          geom(sgp, ps, pd);
          sgp = sgn;
          glds_idx(pb, sgp, 16u, 0u, P4);
          st_write(IC<0>{}, base1);
          const u32x2 xx = ldl<u32x2>(a_x); a_x += 256;
          const u32 t4 = min((sg0 >> 16) - bh, 4u);
          const u32x2 oh = ldl<u32x2>(a_lut + t4 * 8);
          XA0 = f32x16{}; XA1 = f32x16{};
#pragma unroll
          for (int s = 0; s < 4; s++) { XA0 = MFMA(A[s], Bw[s][0], XA0); XA1 = MFMA(A[s], Bw[s][1], XA1); }
          u32x4 a4, b0, b1;
          a4[0] = oh[0]; a4[1] = oh[1]; a4[2] = xx[0]; a4[3] = xx[1];
          b0[0] = ap[0][0]; b0[1] = ap[0][1]; b0[2] = xw[0][0]; b0[3] = xw[0][1];
          b1[0] = ap[1][0]; b1[1] = ap[1][1]; b1[2] = xw[1][0]; b1[3] = xw[1][1];
          XA0 = MFMA(BF8(a4), BF8(b0), XA0); XA1 = MFMA(BF8(a4), BF8(b1), XA1);
          asm volatile("s_nop 7\n\ts_nop 7" : "+v"(XA0), "+v"(XA1));
        }
        auto step = [&](auto next_tag, auto par_c, f32x16& C0, f32x16& C1, f32x16& N0, f32x16& N1, int t) AI {
          constexpr bool NEXT = decltype(next_tag)::value;
          constexpr int PAR = decltype(par_c)::value;
          if (NEXT) {
            if (__builtin_expect((chg >> (t + 1 - t0)) & 1ull, 0)) {
              base1 = __builtin_amdgcn_readlane(gfv, t + 1 - t0); load_ap(base1); bh = (u32)(base1 + 4 * hh);
            }
          }
          u32x4 M[2][2];
          bf16x8 A[4]; u32x4 sk; u32 sr, sgn; f32x4 ps, pd;
          u32x2 xx, oh;
          s16x4 sf[4];
          f32x2 tt[16];
          SB();
#define XP(p) (((p) >> 2) & 1 ? __builtin_shufflevector(C1, C1, 8 * ((p) >> 3) + 2 * ((p) & 3), 8 * ((p) >> 3) + 2 * ((p) & 3) + 1) \
                              : __builtin_shufflevector(C0, C0, 8 * ((p) >> 3) + 2 * ((p) & 3), 8 * ((p) >> 3) + 2 * ((p) & 3) + 1))
#define MSET(p, v) M[((p) >> 2) & 1][((p) >> 3) & 1][(p) & 3] = (v)
          static_for<0, 20>([&](auto ic) AI {
            constexpr int k = decltype(ic)::value - 1;
            constexpr bool R = (k - 1 >= 0 && k - 1 < 16), Cv = (k - 3 >= 0 && k - 3 < 16), E = (k + 1 < 16), Ad = (k >= 0 && k < 16), Mu = (k - 2 >= 0 && k - 2 < 16);
            if constexpr (R && Cv && E && Ad && Mu) {
              const f32x2 xe = XP(k + 1), xm = XP(k - 2);
              u32 mo;
              asm volatile("v_rcp_f32 %0, %0\n\tv_rcp_f32 %1, %1\n\tv_cvt_pk_bf16_f32 %2, %7, %8\n\tv_exp_f32 %3, %9\n\tv_exp_f32 %4, %10\n\t"
                           "v_pk_add_f32 %5, %5, 1.0 op_sel_hi:[1,0]\n\tv_pk_mul_f32 %6, %11, %6"
                           : "+v"(tt[k - 1][0]), "+v"(tt[k - 1][1]), "=&v"(mo), "=&v"(tt[k + 1][0]), "=&v"(tt[k + 1][1]), "+v"(tt[k]), "+v"(tt[k - 2])
                           : "v"(tt[k - 3][0]), "v"(tt[k - 3][1]), "v"(xe[0]), "v"(xe[1]), "v"(xm));
              MSET(k - 3, mo);
            } else {
              if constexpr (R) asm volatile("v_rcp_f32 %0, %0\n\tv_rcp_f32 %1, %1" : "+v"(tt[(k - 1) & 15][0]), "+v"(tt[(k - 1) & 15][1]));
              if constexpr (Cv) {
                u32 mo;
                if constexpr (k == 18) asm volatile("v_cvt_pk_bf16_f32 %0, %1, %2\n\ts_nop 1" : "=v"(mo) : "v"(tt[(k - 3) & 15][0]), "v"(tt[(k - 3) & 15][1]));
                else asm volatile("v_cvt_pk_bf16_f32 %0, %1, %2" : "=v"(mo) : "v"(tt[(k - 3) & 15][0]), "v"(tt[(k - 3) & 15][1]));
                MSET((k - 3) & 15, mo);
              }
              if constexpr (E) {
                const f32x2 xe = XP((k + 1) & 15);
                asm volatile("v_exp_f32 %0, %2\n\tv_exp_f32 %1, %3" : "=&v"(tt[(k + 1) & 15][0]), "=&v"(tt[(k + 1) & 15][1]) : "v"(xe[0]), "v"(xe[1]));
              }
              if constexpr (Ad) asm volatile("v_pk_add_f32 %0, %0, 1.0 op_sel_hi:[1,0]" : "+v"(tt[k & 15]));
              if constexpr (Mu) {
                const f32x2 xm = XP((k - 2) & 15);
                asm volatile("v_pk_mul_f32 %0, %1, %0" : "+v"(tt[(k - 2) & 15]) : "v"(xm));
              }
            }
            SB();
            if constexpr (k == -1) {
#pragma unroll
              for (int u = 0; u < 4; u++) sf[u] = trl(a_st + 512 * u);
              if constexpr (NEXT) {
                if constexpr (PAR == 1) asm volatile("s_waitcnt vmcnt(1)" ::: "memory"); else asm volatile("s_waitcnt vmcnt(0)" ::: "memory");
#pragma unroll
                for (int s = 0; s < 4; s++) A[s] = ldl<bf16x8>(aoff[s]);
                xx = ldl<u32x2>(a_x);
                oh = ldl<u32x2>(a_lut + min((sg_cur >> 16) - bh, 4u) * 8);
                sk_read(a_sk, sk);
                sr = ldl<u32>(a_sr);
              }
            }
            if constexpr (k == 1 && NEXT) {
              asm volatile("s_waitcnt lgkmcnt(0)" ::: "memory");
              issue_h(sk);
              stl<u32x2>(st_prev, u32x2{0u, 0u});
              st_write(IC<1 - PAR>{}, base1);
            }
            if constexpr (NEXT && k >= 2 && k < 12) {
              constexpr int m = k - 2, ks = m >> 1;
              if constexpr ((m & 1) == 0) {
                if constexpr (ks == 0) N0 = MFMA(A[0], Bw[0][0], f32x16{});
                else if constexpr (ks < 4) N0 = MFMA(A[ks & 3], Bw[ks & 3][0], N0);
                else { u32x4 a4, b0; a4[0] = oh[0]; a4[1] = oh[1]; a4[2] = xx[0]; a4[3] = xx[1];
                       b0[0] = ap[0][0]; b0[1] = ap[0][1]; b0[2] = xw[0][0]; b0[3] = xw[0][1]; N0 = MFMA(BF8(a4), BF8(b0), N0); }
              } else {
                if constexpr (ks == 0) N1 = MFMA(A[0], Bw[0][1], f32x16{});
                else if constexpr (ks < 4) N1 = MFMA(A[ks & 3], Bw[ks & 3][1], N1);
                else { u32x4 a4, b1; a4[0] = oh[0]; a4[1] = oh[1]; a4[2] = xx[0]; a4[3] = xx[1];
                       b1[0] = ap[1][0]; b1[1] = ap[1][1]; b1[2] = xw[1][0]; b1[3] = xw[1][1]; N1 = MFMA(BF8(a4), BF8(b1), N1); }
              }
            }
            if constexpr (k == 12) {
              Y0 = MFMA(mk8(sf[0], sf[1]), BF8(M[0][0]), Y0);
              if constexpr (NEXT && PAR == 0) {
                ps = ldl<f32x4>(a_ps);
                pd = ldl<f32x4>(a_pd + __builtin_amdgcn_ubfe(sgp, 16, 7) * 16);
                sgn = ldl<u32>(a_sp);
              }
            }
            if constexpr (k == 13) Y1 = MFMA(mk8(sf[0], sf[1]), BF8(M[1][0]), Y1);
            if constexpr (k == 14 && NEXT && PAR == 0) {
              geom(sgp, ps, pd);
              sgp = sgn; a_sp += 256; PIN(a_sp);
              asm volatile("s_waitcnt lgkmcnt(0)" ::: "memory");
              glds_idx(pb, sgp, 16u, 0u, P4);
            }
            if constexpr (k == 15 && NEXT) {
              sg_cur = sr; a_sk += 128; a_sr += 128; a_x += (PAR == 0 ? incB : 256u); PIN(a_sk); PIN(a_sr); PIN(a_x);
            }
            if constexpr (k == 16) Y0 = MFMA(mk8(sf[2], sf[3]), BF8(M[0][1]), Y0);
            SB();
          });
          Y1 = MFMA(mk8(sf[2], sf[3]), BF8(M[1][1]), Y1);
          SB();
          if (NEXT) { if (__builtin_expect(base1 != base2, 0)) { flush(base2); base2 = base1; } }
        };
        using Tt = std::integral_constant<bool, true>;
        using Ff = std::integral_constant<bool, false>;
        int t = t0;
        while (t + 2 < t1) { step(Tt{}, IC<0>{}, XA0, XA1, XB0, XB1, t); step(Tt{}, IC<1>{}, XB0, XB1, XA0, XA1, t + 1); t += 2; }
        if (t + 1 < t1) { step(Tt{}, IC<0>{}, XA0, XA1, XB0, XB1, t); step(Ff{}, IC<1>{}, XB0, XB1, XA0, XA1, t + 1); }
        else step(Ff{}, IC<0>{}, XA0, XA1, XB0, XB1, t);
        flush(base2);
        asm volatile("s_waitcnt vmcnt(0)" ::: "memory");
      } else {
        u32 slo, slen;
        auto load_sel = [&](int bs) AI {
          const int nl = bs + 4 * blk + tp;
          slo = off[nl]; slen = off[nl + 1] - slo;
        };
        const u32 a_ps = pb + r * 16;
        load_ap(base); load_sel(base);
        u32 sg_cur = ldl<u32>(a_sr); a_sr += 128;
        glds_idx_lo32(pb, sg_cur, P4);
        for (int t = t0; t < t1; t++) {
          const int Tb = t * 32;
          const u32x2 ea = *(const u32x2*)(lds + L_EA + (Tb + r) * 8);
          const f32x4 pd = *(const f32x4*)(lds + L_PD + ((sg_cur >> 16) & 127u) * 16);
          u32x4 sk; sk_read(a_sk, sk);
          const u32 sr = ldl<u32>(a_sr);
          asm volatile("s_waitcnt vmcnt(0)" ::: "memory");
          bf16x8 A[4];
#pragma unroll
          for (int s = 0; s < 4; s++) A[s] = ldl<bf16x8>(aoff[s]);
          const f32x4 ps = ldl<f32x4>(a_ps);
          asm volatile("s_waitcnt lgkmcnt(0)" ::: "memory");
          issue_h(sk);
          glds_idx_lo32(pb, sr, P4);
          const u32 sg = sg_cur;
          const float dx = pd[0] - ps[0], dy = pd[1] - ps[1], dz = pd[2] - ps[2];
          { u32x2 v; v[0] = pk_bf16(1.0f, dx); v[1] = pk_bf16(dy, dz); stl<u32x2>(a_vw, v); }
          const u32 d2 = pk_bf16(dx * dx + dy * dy + dz * dz, 0.f);
          sg_cur = sr; a_sk += 128; a_sr += 128;
          const int g = min((int)(sg >> 16), glast);
          const int gf = __builtin_amdgcn_readlane(g, 0), gl = __builtin_amdgcn_readlane(g, 31);
          if (gl >= base + 8) { flush(base); base = gf; load_ap(base); load_sel(base); }
          f32x16 X0 = {}, X1 = {};
#pragma unroll
          for (int s = 0; s < 4; s++) { X0 = MFMA(A[s], Bw[s][0], X0); X1 = MFMA(A[s], Bw[s][1], X1); }
          for (int wb = base;; wb += 8) {
            if (wb != base) load_ap(wb);
            const u32 t4 = (u32)g - (u32)(wb + 4 * hh);
            unsigned long long oh = 0x3F80ull << ((t4 & 3u) * 16u);
            if (t4 >= 4u) oh = 0ull;
            u32x4 a4; a4[0] = (u32)oh; a4[1] = (u32)(oh >> 32);
            a4[2] = wb == base ? (hh ? d2 : ea[0]) : 0u; a4[3] = wb == base ? (hh ? d2 : ea[1]) : 0u;
            u32x4 b0, b1;
            b0[0] = ap[0][0]; b0[1] = ap[0][1]; b0[2] = xw[0][0]; b0[3] = xw[0][1];
            b1[0] = ap[1][0]; b1[1] = ap[1][1]; b1[2] = xw[1][0]; b1[3] = xw[1][1];
            X0 = MFMA(BF8(a4), BF8(b0), X0);
            X1 = MFMA(BF8(a4), BF8(b1), X1);
            if (gl < wb + 8) { if (wb != base) load_ap(base); break; }
          }
          u32x4 M[2][2];
#pragma unroll
          for (int s = 0; s < 2; s++)
#pragma unroll
            for (int j = 0; j < 4; j++) {
              M[0][s][j] = silu2(X0[8 * s + 2 * j], X0[8 * s + 2 * j + 1]);
              M[1][s][j] = silu2(X1[8 * s + 2 * j], X1[8 * s + 2 * j + 1]);
            }
          for (;;) {
            s16x4 sf[4];
            const u32 d0 = (u32)(Tb + 4 * hh + tq) - slo;
#pragma unroll
            for (int u = 0; u < 4; u++) sf[u] = trl(((d0 + 8u * u) < slen ? a_vr : a_z) + 64 * u);
#pragma unroll
            for (int s = 0; s < 2; s++) {
              const bf16x8 S = mk8(sf[2 * s], sf[2 * s + 1]);
              Y0 = MFMA(S, BF8(M[0][s]), Y0);
              Y1 = MFMA(S, BF8(M[1][s]), Y1);
            }
            if (gl < base + 8) break;
            flush(base); base += 8; load_ap(base); load_sel(base);
          }
        }
        flush(base);
        asm volatile("s_waitcnt vmcnt(0)" ::: "memory");
      }
    }
    STAMP(1, 7);
    if (lane == 0) misc[16 + slot] = (u32)head;
    BAR();
    {
      float* hb = (float*)(lds + L_HB) + wv * MS;
      const int hprev = slot > 0 ? (int)misc[16 + slot - 1] : 127, hnext = slot < 7 ? (int)misc[16 + slot + 1] : 127;
      const bool uniq = (hprev != head) && (hnext != head);
      if (head != 127) {
        const float v = hb[lane];
        hb[lane] = 0.f;
        if (uniq) macc[head * MS + lane] += v;
        else atomicAdd(&macc[head * MS + lane], v);
      }
      if (tid < 144) hist[tid] = 0;
    }
  };

  load_records(0, 4 * MAXR);
  STAMP(1, 2);
  int Pmax = 0, ntot = 0;
  {
    for (int i = tid; i < (99 + 8) * MS / 4; i += 512) *(f32x4*)(macc + 4 * i) = f32x4{0.f, 0.f, 0.f, 0.f};
    if (tid < 105) *(f32x4*)(pacc + 4 * tid) = f32x4{0.f, 0.f, 0.f, 0.f};
    if (tid < 64) *(u32*)(lds + L_ZERO + tid * 4) = 0;
    if (tid < 5) { u32x2 v; v[0] = tid == 0 ? 0x3F80u : (tid == 1 ? 0x3F800000u : 0u); v[1] = tid == 2 ? 0x3F80u : (tid == 3 ? 0x3F800000u : 0u); *(u32x2*)(lds + L_LUT + tid * 8) = v; }
#pragma unroll
    for (int i = 0; i < 2; i++) *(u32x4*)(lds + L_ST + (tid + 512 * i) * 16) = u32x4{0, 0, 0, 0};
    if (tid < 128) { f32x4 v; v[0] = bf2f(pd0); v[1] = bf2f(pd1); v[2] = bf2f(pd2); v[3] = 0.f; *(f32x4*)(lds + L_PD + tid * 16) = v; }
    if (tid < 144) hist[tid] = 0;
    if (lane == 0) { misc[wv] = (u32)P; misc[8 + wv] = (u32)csum; }
    u32x4 o;
#pragma unroll
    for (int i = 0; i < 4; i++) o[i] = pk_bf16(bflo(wB[i]) * NLOG2E, bfhi(wB[i]) * NLOG2E);
    const u32x4 z4 = {0, 0, 0, 0};
    *(u32x4*)(lds + L_WE + tid * 16) = wA;
    *(u32x4*)(lds + L_WE + 8192 + tid * 16) = o;
    if (tid < 40) *(u32x4*)(lds + L_WE + 16384 + tid * 16) = wC;
    *(u32x4*)(lds + L_H2 + tid * 16) = tid < nn * 8 ? hA : z4;
    if (tid < 288) *(u32x4*)(lds + L_H2 + 8192 + tid * 16) = tid + 512 < nn * 8 ? hB : z4;
    BAR();
    STAMP(1, 3);
#pragma unroll
    for (int i = 0; i < 8; i++) { Pmax = max(Pmax, (int)misc[i]); ntot += (int)misc[8 + i]; }
    Pmax = __builtin_amdgcn_readfirstlane(Pmax); ntot = __builtin_amdgcn_readfirstlane(ntot);
#pragma unroll
    for (int s = 0; s < 4; s++)
#pragma unroll
      for (int ct = 0; ct < 2; ct++) {
        const u32 p = lds0 + (u32)(L_WE + (64 + 32 * hh + 8 * s + tq) * 128 + (32 * ct + 16 * blk + 4 * tp) * 2);
        Bw[s][ct] = mk8(trl(p), trl(p + 4 * 128));
      }
#pragma unroll
    for (int ct = 0; ct < 2; ct++) {
      const u16* wt = (const u16*)(lds + L_WE + 16384);
      const int col = 32 * ct + r;
      const float w0 = bf2f(wt[col]) * NLOG2E, w1 = bf2f(wt[64 + col]) * NLOG2E, w2 = bf2f(wt[128 + col]) * NLOG2E,
                  w3 = bf2f(wt[192 + col]) * NLOG2E, w4 = bf2f(wt[256 + col]) * NLOG2E;
      const u32 hi = pk_bf16(w0, 0.f) & 0xffffu;
      const u32 lo = pk_bf16(w0 - bf2f(hi), 0.f) & 0xffffu;
      xw[ct][0] = hh ? hi : pk_bf16(w1, w2);
      xw[ct][1] = hh ? lo : pk_bf16(w3, w4);
    }
    {
      const int nt = wv >> 1, ct = wv & 1;
      f32x16 acc = {};
#pragma unroll
      for (int s = 0; s < 4; s++) {
        const u32 p = lds0 + (u32)(L_WE + (32 * hh + 8 * s + tq) * 128 + (32 * ct + 16 * blk + 4 * tp) * 2);
        bf16x8 aw = mk8(trl(p), trl(p + 4 * 128));
        bf16x8 bh = *(const bf16x8*)(lds + L_H2 + min(32 * nt + r, 99) * 128 + (4 * hh + s) * 16);
        acc = MFMA(aw, bh, acc);
      }
      const int node = 32 * nt + r;
      char* ap = lds + (node < 105 ? L_AP + node * 128 : L_DESC + 2048 + wv * 128) + (32 * ct + 4 * hh) * 2;
#pragma unroll
      for (int q = 0; q < 4; q++) {
        u32x2 v;
        v[0] = pk_bf16(acc[4 * q] * NLOG2E, acc[4 * q + 1] * NLOG2E);
        v[1] = pk_bf16(acc[4 * q + 2] * NLOG2E, acc[4 * q + 3] * NLOG2E);
        *(u32x2*)(ap + 16 * q) = v;
      }
    }
  }
  if (Pmax <= 4 * MAXR && ntot <= CAP) {
    process(std::integral_constant<bool, true>{});
  } else {
    for (int plo = 0; plo < Pmax; plo += FPPB) {
      BAR();
      load_records(plo, FPPB);
      process(std::integral_constant<bool, false>{});
    }
  }
  STAMP(1, 8);

  {
    const u32x4 uA = wh16[tid], uB = wh16[512 + tid];
    *(u32x4*)(lds + L_WH + tid * 16) = uA;
    *(u32x4*)(lds + L_WH + 8192 + tid * 16) = uB;
  }
  BAR();
  {
    int ln = lane;
    asm volatile("" : "+v"(ln));
    const int r = ln & 31, hh = ln >> 5;
    const int G4 = ln >> 4, i16 = ln & 15, tq = i16 >> 2, tp = i16 & 3, blk = G4 & 1;
    const int nt = wv >> 1, ct = wv & 1;
    const int node = 32 * nt + r;
    const int nrow = node < NB ? node : MDUMMY;
    f32x16 acc = {};
#pragma unroll
    for (int s = 0; s < 8; s++) {
      const int kb = (s < 4) ? (32 * hh + 8 * s) : (64 + 32 * hh + 8 * (s - 4));
      const u32 p = lds0 + (u32)(L_WH + (kb + tq) * 128 + (32 * ct + 16 * blk + 4 * tp) * 2);
      const bf16x8 aw = mk8(trl(p), trl(p + 4 * 128));
      bf16x8 bb;
      if (s < 4) {
        bb = *(const bf16x8*)(lds + L_H2 + min(node, 99) * 128 + (4 * hh + s) * 16);
      } else {
        const float* mp = macc + nrow * MS + 32 * hh + 8 * (s - 4);
        const f32x4 m0 = *(const f32x4*)mp, m1 = *(const f32x4*)(mp + 4);
        u32x4 pk;
        pk[0] = pk_bf16(m0[0] * NLN2, m0[1] * NLN2); pk[1] = pk_bf16(m0[2] * NLN2, m0[3] * NLN2);
        pk[2] = pk_bf16(m1[0] * NLN2, m1[1] * NLN2); pk[3] = pk_bf16(m1[2] * NLN2, m1[3] * NLN2);
        bb = __builtin_bit_cast(bf16x8, pk);
      }
      acc = MFMA(aw, bb, acc);
    }
#pragma unroll
    for (int q = 0; q < 4; q++) {
      float u[4];
#pragma unroll
      for (int i = 0; i < 4; i++) {
        const float x = bf2f(pk_bf16(acc[4 * q + i], 0.f) & 0xffffu);
        u[i] = x * __builtin_amdgcn_rcpf(1.0f + __builtin_amdgcn_exp2f(x * NLOG2E));
      }
      u32x2 v; v[0] = pk_bf16(u[0], u[1]); v[1] = pk_bf16(u[2], u[3]);
      *(u32x2*)(lds + L_UT + node * 144 + (32 * ct + 8 * q + 4 * hh) * 2) = v;
    }
  }
  BAR();
#pragma unroll
  for (int k = 0; k < 2; k++) {
    const int i = tid + 512 * k;
    if (i < nn * 8) {
      const int row = i >> 3, chn = i & 7;
      const u32x4 u = *(const u32x4*)(lds + L_UT + row * 144 + chn * 16);
      const u32x4 hv = *(const u32x4*)(lds + L_H2 + i * 16);
      u32x4 o;
#pragma unroll
      for (int j = 0; j < 4; j++) o[j] = pk_bf16(bflo(hv[j]) + bflo(u[j]), bfhi(hv[j]) + bfhi(u[j]));
      *(u32x4*)(out + (size_t)(node0 + row) * 64 + chn * 8) = o;
    }
  }
  if (tid < nn * 3) {
    const int nd = tid / 3, c = tid - nd * 3;
    const float pv = *(const float*)(lds + L_PD + nd * 16 + c * 4);
    const float dlt = bf2f(pk_bf16(pacc[nd * 4 + c], 0.f) & 0xffffu);
    out[(size_t)N * 64 + (size_t)node0 * 3 + tid] = (u16)(pk_bf16(pv + dlt, 0.f) & 0xffffu);
  }
  STAMP(1, 9);
  STAMP_OUT(1, R);
}

extern "C" void kernel_launch(void* const* d_in, const int* in_sizes, int n_in, void* d_out, int out_size, void* d_ws,
                              size_t ws_size, hipStream_t stream) {
  const u16* h = (const u16*)d_in[0];
  const u16* pos = (const u16*)d_in[1];
  const u16* ea = (const u16*)d_in[2];
  const int* src = (const int*)d_in[3];
  const int* dst = (const int*)d_in[4];
  const u16* we = (const u16*)d_in[5];
  const u16* wx = (const u16*)d_in[6];
  const u16* wh = (const u16*)d_in[7];
  const int N = in_sizes[0] / 64, E = in_sizes[3];
  char* ws = (char*)d_ws;
  u32* R = (u32*)ws;
  f32x4* P4 = (f32x4*)(ws + (size_t)10 * 1048576);
  u32* T = (u32*)(ws + (size_t)11 * 1048576);
  (void)hipFuncSetAttribute((const void*)k1_partition, hipFuncAttributeMaxDynamicSharedMemorySize, K1_LDS);
  (void)hipFuncSetAttribute((const void*)k2_main, hipFuncAttributeMaxDynamicSharedMemorySize, K2_LDS);
  k1_partition<<<(E + CH - 1) / CH, 1024, K1_LDS, stream>>>(pos, (const u32x4*)ea, (const i32x4*)src, (const i32x4*)dst, R, P4, T, N, E);
  k2_main<<<(N + NB - 1) / NB, 512, K2_LDS, stream>>>(h, pos, we, wx, wh, R, P4, T, (u16*)d_out, N);
}
```

```cpp
#include <hip/hip_runtime.h>
#include <stdint.h>
#include <type_traits>
#define STV 0
#define LDV 1
#define WARMH 0
#define WNT 0

typedef unsigned short u16;
typedef unsigned char u8;
typedef unsigned int u32;
typedef short s16x4 __attribute__((ext_vector_type(4)));
typedef __bf16 bf16x2 __attribute__((ext_vector_type(2)));
typedef __bf16 bf16x8 __attribute__((ext_vector_type(8)));
typedef float f32x2 __attribute__((ext_vector_type(2)));
typedef float f32x4 __attribute__((ext_vector_type(4)));
typedef float f32x16 __attribute__((ext_vector_type(16)));
typedef u32 u32x2 __attribute__((ext_vector_type(2)));
typedef u32 u32x3 __attribute__((ext_vector_type(3)));
typedef u32 u32x4 __attribute__((ext_vector_type(4)));
typedef int i32x4 __attribute__((ext_vector_type(4)));
#define LAS __attribute__((address_space(3)))
#define DI __device__ __forceinline__
#define AI __attribute__((always_inline))

constexpr int NB    = 98;
constexpr int CH    = 3128;
constexpr int NCH   = 256;
constexpr int CAP   = 3392;
constexpr int MAXR  = 12;
constexpr int FPPB  = 26;
#define SPLIT 86
constexpr float NLOG2E = -1.4426950408889634f;
constexpr float NLN2   = -0.6931471805599453f;

DI float bf2f(u32 bits16) { return __uint_as_float(bits16 << 16); }
DI float bflo(u32 v) { return __uint_as_float(v << 16); }
DI float bfhi(u32 v) { return __uint_as_float(v & 0xffff0000u); }
DI u32 pk_bf16(float lo, float hi) {
  f32x2 v; v[0] = lo; v[1] = hi;
  return __builtin_bit_cast(u32, __builtin_convertvector(v, bf16x2));
}
template <int CTRL, int RMASK> DI int dpp_i(int x) { return __builtin_amdgcn_update_dpp(0, x, CTRL, RMASK, 0xf, false); }
template <int CTRL> DI float dpp_f(float x) {
  return __builtin_bit_cast(float, __builtin_amdgcn_update_dpp(0, __builtin_bit_cast(int, x), CTRL, 0xf, 0xf, true));
}
DI int wave_incl_scan(int x) {
  x += dpp_i<0x111, 0xf>(x); x += dpp_i<0x112, 0xf>(x); x += dpp_i<0x114, 0xf>(x); x += dpp_i<0x118, 0xf>(x);
  x += dpp_i<0x142, 0xa>(x); x += dpp_i<0x143, 0xc>(x);
  return x;
}
DI float half_reduce(float x) {
  x += dpp_f<0x111>(x); x += dpp_f<0x112>(x); x += dpp_f<0x114>(x); x += dpp_f<0x118>(x);
  x += dpp_f<0x142>(x);
  return x;
}
DI void glds16(u32 lds_addr, u32 voff, const void* base) {
  asm volatile("s_mov_b32 m0, %0\n\ts_nop 0\n\tglobal_load_lds_dwordx4 %1, %2" ::"s"(lds_addr), "v"(voff), "s"(base) : "memory");
}
DI void glds16_lo32(u32 lds_addr, u32 voff, const void* base) {
  asm volatile("s_mov_b32 m0, %0\n\ts_mov_b64 exec, 0xffffffff\n\tglobal_load_lds_dwordx4 %1, %2\n\ts_mov_b64 exec, -1"
               ::"s"(lds_addr), "v"(voff), "s"(base) : "memory");
}
DI void glds_idx(u32 lds_addr, u32 idx, u32 mul, u32 add, const void* base) {
  u32 t;
  asm volatile("s_mov_b32 m0, %1\n\tv_mad_u32_u16 %0, %2, %3, %4\n\tglobal_load_lds_dwordx4 %0, %5"
               : "=&v"(t) : "s"(lds_addr), "v"(idx), "s"(mul), "v"(add), "s"(base) : "memory");
}
DI void glds_idx_lo32(u32 lds_addr, u32 idx, const void* base) {
  u32 t;
  asm volatile("s_mov_b32 m0, %1\n\tv_mad_u32_u16 %0, %2, 16, 0\n\ts_mov_b64 exec, 0xffffffff\n\tglobal_load_lds_dwordx4 %0, %3\n\ts_mov_b64 exec, -1"
               : "=&v"(t) : "s"(lds_addr), "v"(idx), "s"(base) : "memory");
}
DI u32 mad16(u32 a_lo16, u32 b, u32 c) {
  u32 d;
  asm("v_mad_u32_u16 %0, %1, %2, %3" : "=v"(d) : "v"(a_lo16), "s"(b), "v"(c));
  return d;
}
template <int HALF> DI void half_write_b64(u32& prev, u32 a, u32x2 v) {
  if constexpr (HALF == 0)
    asm volatile("s_mov_b64 exec, 0xffffffff\n\tds_write_b64 %1, %2\n\tv_mov_b32 %0, %1\n\ts_mov_b64 exec, -1" : "+v"(prev) : "v"(a), "v"(v) : "memory");
  else
    asm volatile("s_mov_b64 exec, %3\n\tds_write_b64 %1, %2\n\tv_mov_b32 %0, %1\n\ts_mov_b64 exec, -1" : "+v"(prev) : "v"(a), "v"(v), "s"(0xffffffff00000000ull) : "memory");
}
DI void reduce12(float (&v)[12], float& x0, float& x1) {
  asm volatile(
      "s_nop 1\n\t"
      "v_permlane16_swap_b32 %0, %1\n\tv_permlane16_swap_b32 %2, %3\n\tv_permlane16_swap_b32 %4, %5\n\t"
      "v_permlane16_swap_b32 %6, %7\n\tv_permlane16_swap_b32 %8, %9\n\tv_permlane16_swap_b32 %10, %11\n\t"
      "s_nop 1\n\t"
      "v_add_f32 %0, %0, %1\n\tv_add_f32 %2, %2, %3\n\tv_add_f32 %4, %4, %5\n\t"
      "v_add_f32 %6, %6, %7\n\tv_add_f32 %8, %8, %9\n\tv_add_f32 %10, %10, %11\n\t"
      "v_add_f32_dpp %1, %0, %0 row_ror:8 row_mask:0xf bank_mask:0x3\n\t"
      "v_add_f32_dpp %3, %4, %4 row_ror:8 row_mask:0xf bank_mask:0x3\n\t"
      "v_add_f32_dpp %5, %8, %8 row_ror:8 row_mask:0xf bank_mask:0x3\n\t"
      "v_add_f32_dpp %1, %2, %2 row_ror:8 row_mask:0xf bank_mask:0xc\n\t"
      "v_add_f32_dpp %3, %6, %6 row_ror:8 row_mask:0xf bank_mask:0xc\n\t"
      "v_add_f32_dpp %5, %10, %10 row_ror:8 row_mask:0xf bank_mask:0xc\n\t"
      "v_add_f32_dpp %7, %1, %1 row_half_mirror row_mask:0xf bank_mask:0x5\n\t"
      "v_add_f32_dpp %7, %3, %3 row_half_mirror row_mask:0xf bank_mask:0xa\n\t"
      "v_add_f32_dpp %9, %5, %5 row_half_mirror row_mask:0xf bank_mask:0xf\n\t"
      "s_nop 1\n\t"
      "v_add_f32_dpp %7, %7, %7 quad_perm:[1,0,3,2] row_mask:0xf bank_mask:0xf\n\t"
      "v_add_f32_dpp %9, %9, %9 quad_perm:[1,0,3,2] row_mask:0xf bank_mask:0xf\n\t"
      "s_nop 1\n\t"
      "v_add_f32_dpp %7, %7, %7 quad_perm:[2,3,0,1] row_mask:0xf bank_mask:0xf\n\t"
      "v_add_f32_dpp %9, %9, %9 quad_perm:[2,3,0,1] row_mask:0xf bank_mask:0xf\n\t"
      "s_nop 0"
      : "+v"(v[0]), "+v"(v[1]), "+v"(v[2]), "+v"(v[3]), "+v"(v[4]), "+v"(v[5]), "+v"(v[6]), "+v"(v[7]), "+v"(v[8]), "+v"(v[9]),
        "+v"(v[10]), "+v"(v[11]));
  x0 = v[7]; x1 = v[9];
}
DI void masked_add2(u32 a0, float x0, u32 a1, float x1) {
  asm volatile("s_mov_b64 exec, %4\n\tds_add_f32 %0, %1\n\ts_mov_b64 exec, %5\n\tds_add_f32 %2, %3\n\ts_mov_b64 exec, -1"
               ::"v"(a0), "v"(x0), "v"(a1), "v"(x1), "s"(0x1111111111111111ull), "s"(0x0101010101010101ull) : "memory");
}
template <class T> DI T ldl(u32 a) { return *(const LAS T*)(uintptr_t)a; }
template <class T> DI void stl(u32 a, T v) { *(LAS T*)(uintptr_t)a = v; }
DI s16x4 trl(u32 a) { return __builtin_amdgcn_ds_read_tr16_b64_v4i16((LAS s16x4*)(uintptr_t)a); }
DI bf16x8 mk8(s16x4 lo, s16x4 hi) {
  return __builtin_bit_cast(bf16x8, __builtin_shufflevector(lo, hi, 0, 1, 2, 3, 4, 5, 6, 7));
}
#define BAR() do { asm volatile("s_waitcnt lgkmcnt(0)" ::: "memory"); __builtin_amdgcn_s_barrier(); asm volatile("" ::: "memory"); } while (0)
#define SB() __builtin_amdgcn_sched_barrier(0)
#define PIN(x) asm volatile("" : "+v"(x))
#define STAMP(k,i)
#define STAMP_DECL
#define STAMP_OUT(k,base)
template <int I, int N, class F> DI void static_for(F&& f) {
  if constexpr (I < N) { f(std::integral_constant<int, I>{}); static_for<I + 1, N>(f); }
}
template <int I> using IC = std::integral_constant<int, I>;
#define MFMA(a, b, c) __builtin_amdgcn_mfma_f32_32x32x16_bf16((a), (b), (c), 0, 0, 0)
#define BF8(x) __builtin_bit_cast(bf16x8, (x))

constexpr int K1_HIST = CH * 12 + 64;
constexpr int K1_OFF = K1_HIST + 1024 + 64;
constexpr int K1_LDS = K1_OFF + 1024 + 64;

__global__ __launch_bounds__(1024) void k1_partition(const u32x4* __restrict__ h16, const u16* __restrict__ pos, const u32x4* __restrict__ ea4,
                                                     const i32x4* __restrict__ src4, const i32x4* __restrict__ dst4,
                                                     u32* __restrict__ R, f32x4* __restrict__ P4, u32* __restrict__ T,
                                                     int N, int E) {
  extern __shared__ __attribute__((aligned(16))) char lds[];
  const int tid = threadIdx.x, lane = tid & 63;
  const int wv = __builtin_amdgcn_readfirstlane(tid >> 6);
  const int w = blockIdx.x;
  u32* hist = (u32*)(lds + K1_HIST);
  u32* off = (u32*)(lds + K1_OFF);
  STAMP_DECL
  STAMP(0, 0);
  int cnt = E - w * CH; cnt = cnt > CH ? CH : cnt; cnt = cnt < 4 ? 4 : cnt;
  const bool act = 4 * tid < cnt;
  const int q = (w * CH >> 2) + (act ? tid : 0);
  const i32x4 d4 = __builtin_nontemporal_load(&dst4[q]);
  const i32x4 s4 = __builtin_nontemporal_load(&src4[q]);
  const u32x4 a0 = __builtin_nontemporal_load(&ea4[2 * q]);
  const u32x4 a1 = __builtin_nontemporal_load(&ea4[2 * q + 1]);
  {
    const int n = w * NB + (tid - 896);
    if (tid >= 896 && tid < 896 + NB && n < N) {
      const u16* p = pos + (size_t)n * 3;
      f32x4 v; v[0] = bf2f(p[0]); v[1] = bf2f(p[1]); v[2] = bf2f(p[2]); v[3] = 0.f;
      P4[n] = v;
    }
  }
  if (tid < 272) hist[tid] = 0;
  BAR();
  STAMP(0, 1);
  int bk[4], gl[4], rk[4];
#pragma unroll
  for (int i = 0; i < 4; i++) {
    const u32 d = (u32)d4[i];
    bk[i] = (int)((d * 42800u) >> 22);
    gl[i] = (int)d - bk[i] * NB;
    bk[i] = act ? bk[i] : 256 + (lane & 15);
    rk[i] = atomicAdd(&hist[bk[i]], 1u);
  }
  STAMP(0, 2);
  BAR();
  STAMP(0, 3);
  {
    u32x4 c = *(const u32x4*)(hist + 4 * lane);
    int s = c[0] + c[1] + c[2] + c[3];
    int inc = wave_incl_scan(s);
    u32x4 o;
    o[0] = inc - s; o[1] = o[0] + c[0]; o[2] = o[1] + c[1]; o[3] = o[2] + c[2];
    *(u32x4*)(off + 4 * lane) = o;
    if (wv == 15) {
#pragma unroll
      for (int i = 0; i < 4; i++) T[(4 * lane + i) * NCH + w] = o[i] | (c[i] << 16);
    }
  }
  STAMP(0, 4);
  if (act) {
#pragma unroll
    for (int i = 0; i < 4; i++) {
      const int slot = (int)(off[bk[i]] + rk[i]);
      u32* p = (u32*)(lds + slot * 12);
      p[0] = i < 2 ? a0[2 * i] : a1[2 * i - 4];
      p[1] = i < 2 ? a0[2 * i + 1] : a1[2 * i - 3];
      p[2] = (u32)s4[i] | ((u32)gl[i] << 16);
    }
  }
  BAR();
  STAMP(0, 5);
  u32x4* Rw = (u32x4*)(R + (size_t)w * CH * 3);
  const int n16 = (cnt * 12 + 15) >> 4;
#pragma unroll
  for (int k = 0; k < 3; k++) {
    const int i = tid + 1024 * k;
    if (i < n16) __builtin_nontemporal_store(*(const u32x4*)(lds + i * 16), &Rw[i]);
  }
  STAMP(0, 6);
  STAMP_OUT(0, R);
}

constexpr int L_GB = 0;
constexpr int L_PB = L_GB + 8 * 4096;
constexpr int L_D2 = L_PB + 8 * 1024;
constexpr int L_ST = L_D2 + 8 * 512;
constexpr int L_EA = L_ST + 8 * 2048;
constexpr int L_SG = L_EA + (CAP + 32) * 8;
constexpr int SGPAD = 128;
constexpr int L_AP = L_SG + (CAP + 160) * 4;
constexpr int MS = 68;
constexpr int MDUMMY = 98;
constexpr int L_MACC = L_AP + 105 * 128;
constexpr int L_HB = L_MACC + 99 * MS * 4;
constexpr int L_PACC = L_HB + 8 * MS * 4;
constexpr int L_HIST = L_PACC + 105 * 16;
constexpr int L_OFF = L_HIST + 144 * 4;
constexpr int L_ZERO = L_OFF + 144 * 4;
constexpr int L_LUT = L_ZERO + 256;
constexpr int L_PD = L_LUT + 64;
constexpr int L_MISC = L_PD + 128 * 16;
constexpr int L_H2 = L_MISC + 256;
constexpr int K2_LDS = L_H2 + 12800;
static_assert(K2_LDS <= 163840, "LDS");
static_assert(L_AP % 16 == 0 && L_MACC % 16 == 0 && L_EA % 16 == 0 && L_SG % 16 == 0 && L_HB % 16 == 0 && L_PD % 16 == 0 && L_H2 % 16 == 0, "align");
constexpr int L_WE = L_EA;
constexpr int L_DESC = L_GB;
constexpr int L_UT = L_EA;
constexpr int L_WH = L_EA + 128 * 144;

DI u32 silu2(float z0, float z1) {
  f32x2 z; z[0] = z0; z[1] = z1;
  f32x2 t; t[0] = __builtin_amdgcn_exp2f(z0); t[1] = __builtin_amdgcn_exp2f(z1);
  t = t + 1.0f;
  f32x2 r; r[0] = __builtin_amdgcn_rcpf(t[0]); r[1] = __builtin_amdgcn_rcpf(t[1]);
  f32x2 m = z * r;
  return __builtin_bit_cast(u32, __builtin_convertvector(m, bf16x2));
}

__global__ __launch_bounds__(512) void k2_main(const u16* __restrict__ h, const u16* __restrict__ pos,
                                               const u16* __restrict__ we, const u16* __restrict__ wx,
                                               const u16* __restrict__ wh, const u32* __restrict__ R,
                                               const f32x4* __restrict__ P4, const u32* __restrict__ T,
                                               u16* __restrict__ out, int N) {
  extern __shared__ __attribute__((aligned(16))) char lds[];
  const int tid = threadIdx.x, lane = tid & 63;
  const int wv = __builtin_amdgcn_readfirstlane(tid >> 6);
  const int b = blockIdx.x;
  const int node0 = b * NB;
  int nn = N - node0; nn = nn > NB ? NB : nn; nn = nn < 1 ? 1 : nn;
  const int r = lane & 31, hh = lane >> 5;
  const u32 lds0 = (u32)(uintptr_t)(LAS char*)lds;
  u32* hist = (u32*)(lds + L_HIST);
  u32* off = (u32*)(lds + L_OFF);
  float* macc = (float*)(lds + L_MACC);
  float* pacc = (float*)(lds + L_PACC);
  u32* misc = (u32*)(lds + L_MISC);
  STAMP_DECL
  STAMP(1, 0);

  const u32 tv = T[b * NCH + wv * 32 + r];
  const u32x4* we16 = (const u32x4*)we;
  const u32x4 wA = we16[tid], wB = we16[512 + tid];
  const u32x4 wC = we16[1024 + (tid < 40 ? tid : 0)];
  const u32x4* h16 = (const u32x4*)(h + (size_t)node0 * 64);
  const u32x4 hA = h16[tid < nn * 8 ? tid : 0];
  const u32x4 hB = h16[tid + 512 < nn * 8 ? tid + 512 : 0];
  const u32x4* wh16 = (const u32x4*)wh;
  const u16* pp = pos + (size_t)(node0 + (tid < nn ? tid : 0)) * 3;
  const u32 pd0 = pp[0], pd1 = pp[1], pd2 = pp[2];
  const float wx0 = bf2f(wx[r]) * NLN2, wx1 = bf2f(wx[32 + r]) * NLN2;

  const u32 cstart = tv & 0xffffu;
  const u32 ccnt = lane < 32 ? (tv >> 16) : 0u;
  const int np = (int)((ccnt + 15) >> 4);
  const int pinc = wave_incl_scan(np);
  const int pexc = pinc - np;
  const int P = __builtin_amdgcn_readlane(pinc, 31);
  const int csum = __builtin_amdgcn_readlane(wave_incl_scan((int)ccnt), 31);
  STAMP(1, 1);

  const int G4 = lane >> 4, i16 = lane & 15, tq = i16 >> 2, tp = i16 & 3, blk = G4 & 1;
  bf16x8 Bw[4][2];
  u32 xw[2][2];
  u32x3 rec[MAXR];
  u32 vmask = 0;
  int nrounds = 0;
#pragma unroll
  for (int rr = 8; rr < MAXR; rr++) rec[rr] = u32x3{0, 0, 0};
  u32* desc = (u32*)(lds + L_DESC + wv * 256);
  auto load_records = [&](int plo, int ppb) AI {
    int k0 = plo - pexc; k0 = k0 < 0 ? 0 : k0;
    int k1 = plo + ppb - pexc; k1 = k1 > np ? np : k1;
    const u32 gbase = (u32)((wv * 32 + r) * CH) + cstart;
    for (int k = k0; k < k1; k++) {
      u32 nrec = ccnt - 16 * k; nrec = nrec > 16 ? 16 : nrec;
      desc[pexc + k - plo] = (gbase + 16 * k) | (nrec << 24);
    }
    int Pb = P - plo; Pb = Pb < 0 ? 0 : Pb; Pb = Pb > ppb ? ppb : Pb;
    nrounds = (Pb + 3) >> 2;
    vmask = 0;
    auto rounds = [&](auto lo_c, auto hi_c) AI {
      constexpr int lo = decltype(lo_c)::value, hi = decltype(hi_c)::value;
      u32 dd[hi - lo];
#pragma unroll
      for (int rr = lo; rr < hi; rr++) dd[rr - lo] = desc[4 * rr + G4];
#pragma unroll
      for (int rr = lo; rr < hi; rr++) {
        const u32 d = (4 * rr + G4 < Pb) ? dd[rr - lo] : 0u;
        const bool v = (u32)i16 < (d >> 24);
        const u32 gi = (d & 0xffffffu) + (v ? i16 : 0);
        rec[rr] = __builtin_nontemporal_load((const u32x3*)(R + (size_t)gi * 3));
        vmask |= v ? (1u << rr) : 0u;
      }
    };
    rounds(IC<0>{}, IC<8>{});
    if (nrounds > 8) rounds(IC<8>{}, IC<10>{});
    if (nrounds > 10) rounds(IC<10>{}, IC<12>{});
  };
  auto process = [&](auto fast_tag) AI {
    constexpr bool ALLOW_FAST = decltype(fast_tag)::value;
    STAMP(1, 4);
    u32 rk[MAXR];
    auto ranks = [&](auto lo_c, auto hi_c) AI {
      constexpr int lo = decltype(lo_c)::value, hi = decltype(hi_c)::value;
#pragma unroll
      for (int rr = lo; rr < hi; rr++) {
        const u32 g = (vmask >> rr) & 1u ? (rec[rr][2] >> 16) : 128u + (lane & 15);
        rk[rr] = atomicAdd(&hist[g], 1u);
      }
    };
#pragma unroll
    for (int rr = 8; rr < MAXR; rr++) rk[rr] = 0;
    ranks(IC<0>{}, IC<8>{});
    if (nrounds > 8) ranks(IC<8>{}, IC<10>{});
    if (nrounds > 10) ranks(IC<10>{}, IC<12>{});
    BAR();
    STAMP(1, 5);
    int n, glast;
    {
      u32x2 c = *(const u32x2*)(hist + 2 * lane);
      int s = c[0] + c[1];
      int inc = wave_incl_scan(s);
      u32x2 o; o[0] = inc - s; o[1] = o[0] + c[0];
      *(u32x2*)(off + 2 * lane) = o;
      n = __builtin_amdgcn_readlane(inc, 63);
      if (lane < 16) off[128 + lane] = n;
      const unsigned long long m1 = __ballot(c[1] != 0), m0 = __ballot(c[0] != 0);
      const int l1 = m1 ? 63 - __builtin_clzll(m1) : -1, l0 = m0 ? 63 - __builtin_clzll(m0) : -1;
      glast = max(2 * l1 + 1, 2 * l0); glast = glast < 0 ? 0 : glast;
    }
    const int ntiles = (n + 31) >> 5;
    auto scatter = [&](auto lo_c, auto hi_c) AI {
      constexpr int lo = decltype(lo_c)::value, hi = decltype(hi_c)::value;
      u32 ps[hi - lo];
#pragma unroll
      for (int rr = lo; rr < hi; rr++) {
        const bool v = (vmask >> rr) & 1u;
        const u32 g = v ? (rec[rr][2] >> 16) : 128u;
        ps[rr - lo] = off[g] + rk[rr];
      }
#pragma unroll
      for (int rr = lo; rr < hi; rr++) {
        const bool v = (vmask >> rr) & 1u;
        const u32 p = v ? ps[rr - lo] : (u32)(CAP + (lane & 31));
        u32x2 e; e[0] = rec[rr][0]; e[1] = rec[rr][1];
        *(u32x2*)(lds + L_EA + p * 8) = e;
        *(u32*)(lds + L_SG + (v ? p : p + (u32)SGPAD) * 4) = rec[rr][2];
      }
    };
    scatter(IC<0>{}, IC<8>{});
    if (nrounds > 8) scatter(IC<8>{}, IC<10>{});
    if (nrounds > 10) scatter(IC<10>{}, IC<12>{});
    {
      const int ps = n + lane + 64 * (wv & 3);
      if (wv >= 4 && ps < ntiles * 32 + SGPAD) {
        u32x2 z; z[0] = 0; z[1] = 0;
        if (ps < ntiles * 32) *(u32x2*)(lds + L_EA + ps * 8) = z;
        *(u32*)(lds + L_SG + ps * 4) = 127u << 16;
      }
    }
    BAR();
    STAMP(1, 6);

    int ln = lane;
    asm volatile("" : "+v"(ln));
    const int r = ln & 31, hh = ln >> 5;
    const int G4 = ln >> 4, i16 = ln & 15, tq = i16 >> 2, tp = i16 & 3, blk = G4 & 1;
    const int kk = ln >> 3, c8 = ln & 7;
    u32 gcst[4], aoff[4];
#pragma unroll
    for (int i = 0; i < 4; i++) gcst[i] = (u32)((c8 ^ i ^ (((kk >> 1) & 1) << 2)) * 16);
    {
      const int ii = r & 3, k2 = r >> 2;
#pragma unroll
      for (int s = 0; s < 4; s++)
        aoff[s] = lds0 + (u32)(L_GB + wv * 4096 + (ii * 8 + k2) * 128 + (((4 * hh + s) ^ ii ^ (((k2 >> 1) & 1) << 2)) * 16));
    }
    const int slot = (wv & 3) * 2 + (wv >> 2);
    auto cum = [&](int k) { return (ntiles * ((k >> 1) * 128 + (k & 1) * SPLIT)) >> 9; };
    const int t0 = cum(slot), t1 = cum(slot + 1);
    int head = 127;
    if (t0 < t1) {
      const u32 gb = lds0 + L_GB + wv * 4096;
      const u32 pb = lds0 + L_PB + wv * 1024;
      const u32 d2b = lds0 + L_D2 + wv * 512;
      const u32 a_vw = d2b + r * 8;
      const u32 a_vr = d2b + (4 * hh + tq) * 8;
      const u32 a_z = lds0 + L_ZERO;
      auto sk_read = [&](u32 a_k, u32x4& sk) AI {
        sk[0] = ldl<u32>(a_k); sk[1] = ldl<u32>(a_k + 4); sk[2] = ldl<u32>(a_k + 8); sk[3] = ldl<u32>(a_k + 12);
      };
      auto issue_h = [&](const u32x4& sk) AI {
        glds_idx(gb, sk[0], 128u, gcst[0], h);
        glds_idx(gb + 1024, sk[1], 128u, gcst[1], h);
        glds_idx(gb + 2048, sk[2], 128u, gcst[2], h);
        glds_idx(gb + 3072, sk[3], 128u, gcst[3], h);
      };
      u32 a_sk = lds0 + (u32)(L_SG + (t0 * 32 + 4 * kk) * 4), a_sr = lds0 + (u32)(L_SG + (t0 * 32 + r) * 4);
      { u32x4 sk; sk_read(a_sk, sk); issue_h(sk); a_sk += 128; }
      const int tl = (t0 + ln < t1) ? t0 + ln : t1 - 1;
      const int gfv = (int)(*(const u32*)(lds + L_SG + tl * 128) >> 16);
      const int glv = min((int)(*(const u32*)(lds + L_SG + tl * 128 + 124) >> 16), glast);
      const bool wide = __ballot(glv - gfv >= 8) != 0ull || (t1 - t0) > 64;
      int base = __builtin_amdgcn_readlane(gfv, 0);
      if ((int)__builtin_amdgcn_readfirstlane(off[base]) < t0 * 32) head = base;
      const int gspan = __builtin_amdgcn_readlane(glv, 63);
      f32x16 Y0 = {}, Y1 = {};
      u32 ap[2][2];
      auto load_ap = [&](int bs) AI {
#pragma unroll
        for (int ct = 0; ct < 2; ct++) {
          s16x4 v = trl(lds0 + (u32)(L_AP + (bs + 4 * hh + tq) * 128 + (32 * ct + 16 * blk + 4 * tp) * 2));
          u32x2 u = __builtin_bit_cast(u32x2, v);
          ap[ct][0] = u[0]; ap[ct][1] = u[1];
        }
      };
      u32 pa0, pa1;
      {
        const int b4 = (ln >> 4) & 1, b3 = (ln >> 3) & 1, b2 = (ln >> 2) & 1;
        const int j0 = 4 * b2 + 2 * b3 + b4, j1 = 8 + 2 * b3 + b4;
        pa0 = lds0 + (u32)(L_PACC + ((2 * (j0 / 3) + hh) * 4 + (j0 % 3)) * 4);
        pa1 = lds0 + (u32)(L_PACC + ((2 * (j1 / 3) + hh) * 4 + (j1 % 3)) * 4);
      }
      auto flush = [&](int bs) AI {
        u32 ma[4];
        float o0[4], o1[4];
        const u32 mrow = lds0 + (u32)(L_MACC + hh * (MS * 4) + 4 * r) + (u32)(bs * (MS * 4));
        if (bs + 7 <= gspan && (head == 127 || bs > head)) {
#pragma unroll
          for (int q = 0; q < 4; q++) ma[q] = mrow + (u32)(2 * q * (MS * 4));
        } else {
#pragma unroll
          for (int q = 0; q < 4; q++) {
            const int nd = bs + 2 * q + hh;
            u32 a = mrow + (u32)(2 * q * (MS * 4));
            a = nd > gspan ? lds0 + (u32)(L_MACC + MDUMMY * (MS * 4)) + 4 * r : a;
            a = nd == head ? lds0 + (u32)(L_HB + wv * (MS * 4)) + 4 * r : a;
            ma[q] = a;
          }
        }
#pragma unroll
        for (int q = 0; q < 4; q++) { o0[q] = ldl<float>(ma[q]); o1[q] = ldl<float>(ma[q] + 128); }
        float pr[12];
#pragma unroll
        for (int q = 0; q < 4; q++)
#pragma unroll
          for (int c = 1; c < 4; c++) pr[q * 3 + c - 1] = Y0[4 * q + c] * wx0 + Y1[4 * q + c] * wx1;
        SB();
#pragma unroll
        for (int q = 0; q < 4; q++) { stl<float>(ma[q], o0[q] + Y0[4 * q]); stl<float>(ma[q] + 128, o1[q] + Y1[4 * q]); }
        float x0, x1;
        reduce12(pr, x0, x1);
        masked_add2(pa0 + (u32)(bs * 16), x0, pa1 + (u32)(bs * 16), x1);
        const bf16x8 z8 = BF8((u32x4{0u, 0u, 0u, 0u}));
        Y0 = MFMA(z8, z8, f32x16{});
        Y1 = MFMA(z8, z8, f32x16{});
      };

      if (ALLOW_FAST && !wide) {
        int base1 = base, base2 = base;
        unsigned long long chg = 0;
        {
          int bb = base;
          for (int i = 1; i < t1 - t0; i++) {
            const int gl1 = __builtin_amdgcn_readlane(glv, i);
            if (gl1 >= bb + 8) { bb = __builtin_amdgcn_readlane(gfv, i); chg |= 1ull << i; }
          }
        }
        load_ap(base1);
        u32 bh = (u32)(base1 + 4 * hh);
        f32x16 XA0, XA1, XB0, XB1;
        const u32 a_ps = pb + ln * 16;
        const u32 a_d2w = d2b + ln * 8;
        const u32 st_row = lds0 + (u32)(L_ST + wv * 2048 + r * 64);
        const u32 a_st = lds0 + (u32)(L_ST + wv * 2048 + (4 * hh + tq) * 64 + (4 * blk + tp) * 8);
        u32 a_pd = lds0 + L_PD, a_lut = lds0 + L_LUT;
        PIN(a_pd); PIN(a_lut);
        u32 a_sp = lds0 + (u32)(L_SG + (t0 * 32 + ln) * 4);
        u32 a_x = hh ? d2b + r * 8 : lds0 + (u32)(L_EA + (t0 * 32 + r) * 8);
        const u32 incB = hh ? (u32)-256 : 256u;
        u32 st_prev = st_row, st_g;
        u32x2 vv;
        u32 sgp;
        auto geom = [&](u32 sg, const f32x4& ps, const f32x4& pd) AI {
          const float dx = pd[0] - ps[0], dy = pd[1] - ps[1], dz = pd[2] - ps[2];
          const bool pad = sg >= (127u << 16);
          u32 g0 = pk_bf16(1.0f, dx), g1 = pk_bf16(dy, dz); PIN(g0); PIN(g1);
          vv[0] = pad ? 0u : g0; vv[1] = pad ? 0u : g1;
          u32x2 d; d[0] = pk_bf16(dx * dx + dy * dy + dz * dz, 0.f); d[1] = d[0];
          stl<u32x2>(a_d2w, d);
          st_g = st_row + (sg >> 16) * 8;
        };
        auto st_write = [&](auto half_c, int bs) AI {
          constexpr int HALF = decltype(half_c)::value;
          u32 a = min(st_g - (u32)(bs * 8), st_row + 56u);
          half_write_b64<HALF>(st_prev, a, vv);
        };
        u32 sg_cur;
        {
          sgp = ldl<u32>(a_sp); a_sp += 256;
          glds_idx(pb, sgp, 16u, 0u, P4);
          const u32 sg0 = ldl<u32>(a_sr); a_sr += 128;
          const f32x4 pd = ldl<f32x4>(a_pd + __builtin_amdgcn_ubfe(sgp, 16, 7) * 16);
          const u32 sgn = ldl<u32>(a_sp); a_sp += 256;
          u32x4 sk; sk_read(a_sk, sk); a_sk += 128;
          sg_cur = ldl<u32>(a_sr); a_sr += 128;
          asm volatile("s_waitcnt vmcnt(0)" ::: "memory");
          bf16x8 A[4];
#pragma unroll
          for (int s = 0; s < 4; s++) A[s] = ldl<bf16x8>(aoff[s]);
          const f32x4 ps = ldl<f32x4>(a_ps);
          asm volatile("s_waitcnt lgkmcnt(0)" ::: "memory");
          issue_h(sk);
          geom(sgp, ps, pd);
          sgp = sgn;
          glds_idx(pb, sgp, 16u, 0u, P4);
          st_write(IC<0>{}, base1);
          const u32x2 xx = ldl<u32x2>(a_x); a_x += 256;
          const u32 t4 = min((sg0 >> 16) - bh, 4u);
          const u32x2 oh = ldl<u32x2>(a_lut + t4 * 8);
          XA0 = f32x16{}; XA1 = f32x16{};
#pragma unroll
          for (int s = 0; s < 4; s++) { XA0 = MFMA(A[s], Bw[s][0], XA0); XA1 = MFMA(A[s], Bw[s][1], XA1); }
          u32x4 a4, b0, b1;
          a4[0] = oh[0]; a4[1] = oh[1]; a4[2] = xx[0]; a4[3] = xx[1];
          b0[0] = ap[0][0]; b0[1] = ap[0][1]; b0[2] = xw[0][0]; b0[3] = xw[0][1];
          b1[0] = ap[1][0]; b1[1] = ap[1][1]; b1[2] = xw[1][0]; b1[3] = xw[1][1];
          XA0 = MFMA(BF8(a4), BF8(b0), XA0); XA1 = MFMA(BF8(a4), BF8(b1), XA1);
          asm volatile("s_nop 7\n\ts_nop 7" : "+v"(XA0), "+v"(XA1));
        }
        auto step = [&](auto next_tag, auto par_c, f32x16& C0, f32x16& C1, f32x16& N0, f32x16& N1, int t) AI {
          constexpr bool NEXT = decltype(next_tag)::value;
          constexpr int PAR = decltype(par_c)::value;
          if (NEXT) {
            if (__builtin_expect((chg >> (t + 1 - t0)) & 1ull, 0)) {
              base1 = __builtin_amdgcn_readlane(gfv, t + 1 - t0); load_ap(base1); bh = (u32)(base1 + 4 * hh);
            }
          }
          u32x4 M[2][2];
          bf16x8 A[4]; u32x4 sk; u32 sr, sgn; f32x4 ps, pd;
          u32x2 xx, oh;
          s16x4 sf[4];
          f32x2 tt[16];
          SB();
#define XP(p) (((p) >> 2) & 1 ? __builtin_shufflevector(C1, C1, 8 * ((p) >> 3) + 2 * ((p) & 3), 8 * ((p) >> 3) + 2 * ((p) & 3) + 1) \
                              : __builtin_shufflevector(C0, C0, 8 * ((p) >> 3) + 2 * ((p) & 3), 8 * ((p) >> 3) + 2 * ((p) & 3) + 1))
#define MSET(p, v) M[((p) >> 2) & 1][((p) >> 3) & 1][(p) & 3] = (v)
          static_for<0, 20>([&](auto ic) AI {
            constexpr int k = decltype(ic)::value - 1;
            constexpr bool R = (k - 1 >= 0 && k - 1 < 16), Cv = (k - 3 >= 0 && k - 3 < 16), E = (k + 1 < 16), Ad = (k >= 0 && k < 16), Mu = (k - 2 >= 0 && k - 2 < 16);
            if constexpr (R && Cv && E && Ad && Mu) {
              const f32x2 xe = XP(k + 1), xm = XP(k - 2);
              u32 mo;
              asm volatile("v_rcp_f32 %0, %0\n\tv_rcp_f32 %1, %1\n\tv_cvt_pk_bf16_f32 %2, %7, %8\n\tv_exp_f32 %3, %9\n\tv_exp_f32 %4, %10\n\t"
                           "v_pk_add_f32 %5, %5, 1.0 op_sel_hi:[1,0]\n\tv_pk_mul_f32 %6, %11, %6"
                           : "+v"(tt[k - 1][0]), "+v"(tt[k - 1][1]), "=&v"(mo), "=&v"(tt[k + 1][0]), "=&v"(tt[k + 1][1]), "+v"(tt[k]), "+v"(tt[k - 2])
                           : "v"(tt[k - 3][0]), "v"(tt[k - 3][1]), "v"(xe[0]), "v"(xe[1]), "v"(xm));
              MSET(k - 3, mo);
            } else {
              if constexpr (R) asm volatile("v_rcp_f32 %0, %0\n\tv_rcp_f32 %1, %1" : "+v"(tt[(k - 1) & 15][0]), "+v"(tt[(k - 1) & 15][1]));
              if constexpr (Cv) {
                u32 mo;
                if constexpr (k == 18) asm volatile("v_cvt_pk_bf16_f32 %0, %1, %2\n\ts_nop 1" : "=v"(mo) : "v"(tt[(k - 3) & 15][0]), "v"(tt[(k - 3) & 15][1]));
                else asm volatile("v_cvt_pk_bf16_f32 %0, %1, %2" : "=v"(mo) : "v"(tt[(k - 3) & 15][0]), "v"(tt[(k - 3) & 15][1]));
                MSET((k - 3) & 15, mo);
              }
              if constexpr (E) {
                const f32x2 xe = XP((k + 1) & 15);
                asm volatile("v_exp_f32 %0, %2\n\tv_exp_f32 %1, %3" : "=&v"(tt[(k + 1) & 15][0]), "=&v"(tt[(k + 1) & 15][1]) : "v"(xe[0]), "v"(xe[1]));
              }
              if constexpr (Ad) asm volatile("v_pk_add_f32 %0, %0, 1.0 op_sel_hi:[1,0]" : "+v"(tt[k & 15]));
              if constexpr (Mu) {
                const f32x2 xm = XP((k - 2) & 15);
                asm volatile("v_pk_mul_f32 %0, %1, %0" : "+v"(tt[(k - 2) & 15]) : "v"(xm));
              }
            }
            SB();
            if constexpr (k == -1) {
#pragma unroll
              for (int u = 0; u < 4; u++) sf[u] = trl(a_st + 512 * u);
              if constexpr (NEXT) {
                if constexpr (PAR == 1) asm volatile("s_waitcnt vmcnt(1)" ::: "memory"); else asm volatile("s_waitcnt vmcnt(0)" ::: "memory");
#pragma unroll
                for (int s = 0; s < 4; s++) A[s] = ldl<bf16x8>(aoff[s]);
                xx = ldl<u32x2>(a_x);
                oh = ldl<u32x2>(a_lut + min((sg_cur >> 16) - bh, 4u) * 8);
                sk_read(a_sk, sk);
                sr = ldl<u32>(a_sr);
              }
            }
            if constexpr (k == 1 && NEXT) {
              asm volatile("s_waitcnt lgkmcnt(0)" ::: "memory");
              issue_h(sk);
              stl<u32x2>(st_prev, u32x2{0u, 0u});
              st_write(IC<1 - PAR>{}, base1);
            }
            if constexpr (NEXT && k >= 2 && k < 12) {
              constexpr int m = k - 2, ks = m >> 1;
              if constexpr ((m & 1) == 0) {
                if constexpr (ks == 0) N0 = MFMA(A[0], Bw[0][0], f32x16{});
                else if constexpr (ks < 4) N0 = MFMA(A[ks & 3], Bw[ks & 3][0], N0);
                else { u32x4 a4, b0; a4[0] = oh[0]; a4[1] = oh[1]; a4[2] = xx[0]; a4[3] = xx[1];
                       b0[0] = ap[0][0]; b0[1] = ap[0][1]; b0[2] = xw[0][0]; b0[3] = xw[0][1]; N0 = MFMA(BF8(a4), BF8(b0), N0); }
              } else {
                if constexpr (ks == 0) N1 = MFMA(A[0], Bw[0][1], f32x16{});
                else if constexpr (ks < 4) N1 = MFMA(A[ks & 3], Bw[ks & 3][1], N1);
                else { u32x4 a4, b1; a4[0] = oh[0]; a4[1] = oh[1]; a4[2] = xx[0]; a4[3] = xx[1];
                       b1[0] = ap[1][0]; b1[1] = ap[1][1]; b1[2] = xw[1][0]; b1[3] = xw[1][1]; N1 = MFMA(BF8(a4), BF8(b1), N1); }
              }
            }
            if constexpr (k == 12) {
              Y0 = MFMA(mk8(sf[0], sf[1]), BF8(M[0][0]), Y0);
              if constexpr (NEXT && PAR == 0) {
                ps = ldl<f32x4>(a_ps);
                pd = ldl<f32x4>(a_pd + __builtin_amdgcn_ubfe(sgp, 16, 7) * 16);
                sgn = ldl<u32>(a_sp);
              }
            }
            if constexpr (k == 13) Y1 = MFMA(mk8(sf[0], sf[1]), BF8(M[1][0]), Y1);
            if constexpr (k == 14 && NEXT && PAR == 0) {
              geom(sgp, ps, pd);
              sgp = sgn; a_sp += 256; PIN(a_sp);
              asm volatile("s_waitcnt lgkmcnt(0)" ::: "memory");
              glds_idx(pb, sgp, 16u, 0u, P4);
            }
            if constexpr (k == 15 && NEXT) {
              sg_cur = sr; a_sk += 128; a_sr += 128; a_x += (PAR == 0 ? incB : 256u); PIN(a_sk); PIN(a_sr); PIN(a_x);
            }
            if constexpr (k == 16) Y0 = MFMA(mk8(sf[2], sf[3]), BF8(M[0][1]), Y0);
            SB();
          });
          Y1 = MFMA(mk8(sf[2], sf[3]), BF8(M[1][1]), Y1);
          SB();
          if (NEXT) { if (__builtin_expect(base1 != base2, 0)) { flush(base2); base2 = base1; } }
        };
        using Tt = std::integral_constant<bool, true>;
        using Ff = std::integral_constant<bool, false>;
        int t = t0;
        while (t + 2 < t1) { step(Tt{}, IC<0>{}, XA0, XA1, XB0, XB1, t); step(Tt{}, IC<1>{}, XB0, XB1, XA0, XA1, t + 1); t += 2; }
        if (t + 1 < t1) { step(Tt{}, IC<0>{}, XA0, XA1, XB0, XB1, t); step(Ff{}, IC<1>{}, XB0, XB1, XA0, XA1, t + 1); }
        else step(Ff{}, IC<0>{}, XA0, XA1, XB0, XB1, t);
        flush(base2);
        asm volatile("s_waitcnt vmcnt(0)" ::: "memory");
      } else {
        u32 slo, slen;
        auto load_sel = [&](int bs) AI {
          const int nl = bs + 4 * blk + tp;
          slo = off[nl]; slen = off[nl + 1] - slo;
        };
        const u32 a_ps = pb + r * 16;
        load_ap(base); load_sel(base);
        u32 sg_cur = ldl<u32>(a_sr); a_sr += 128;
        glds_idx_lo32(pb, sg_cur, P4);
        for (int t = t0; t < t1; t++) {
          const int Tb = t * 32;
          const u32x2 ea = *(const u32x2*)(lds + L_EA + (Tb + r) * 8);
          const f32x4 pd = *(const f32x4*)(lds + L_PD + ((sg_cur >> 16) & 127u) * 16);
          u32x4 sk; sk_read(a_sk, sk);
          const u32 sr = ldl<u32>(a_sr);
          asm volatile("s_waitcnt vmcnt(0)" ::: "memory");
          bf16x8 A[4];
#pragma unroll
          for (int s = 0; s < 4; s++) A[s] = ldl<bf16x8>(aoff[s]);
          const f32x4 ps = ldl<f32x4>(a_ps);
          asm volatile("s_waitcnt lgkmcnt(0)" ::: "memory");
          issue_h(sk);
          glds_idx_lo32(pb, sr, P4);
          const u32 sg = sg_cur;
          const float dx = pd[0] - ps[0], dy = pd[1] - ps[1], dz = pd[2] - ps[2];
          { u32x2 v; v[0] = pk_bf16(1.0f, dx); v[1] = pk_bf16(dy, dz); stl<u32x2>(a_vw, v); }
          const u32 d2 = pk_bf16(dx * dx + dy * dy + dz * dz, 0.f);
          sg_cur = sr; a_sk += 128; a_sr += 128;
          const int g = min((int)(sg >> 16), glast);
          const int gf = __builtin_amdgcn_readlane(g, 0), gl = __builtin_amdgcn_readlane(g, 31);
          if (gl >= base + 8) { flush(base); base = gf; load_ap(base); load_sel(base); }
          f32x16 X0 = {}, X1 = {};
#pragma unroll
          for (int s = 0; s < 4; s++) { X0 = MFMA(A[s], Bw[s][0], X0); X1 = MFMA(A[s], Bw[s][1], X1); }
          for (int wb = base;; wb += 8) {
            if (wb != base) load_ap(wb);
            const u32 t4 = (u32)g - (u32)(wb + 4 * hh);
            unsigned long long oh = 0x3F80ull << ((t4 & 3u) * 16u);
            if (t4 >= 4u) oh = 0ull;
            u32x4 a4; a4[0] = (u32)oh; a4[1] = (u32)(oh >> 32);
            a4[2] = wb == base ? (hh ? d2 : ea[0]) : 0u; a4[3] = wb == base ? (hh ? d2 : ea[1]) : 0u;
            u32x4 b0, b1;
            b0[0] = ap[0][0]; b0[1] = ap[0][1]; b0[2] = xw[0][0]; b0[3] = xw[0][1];
            b1[0] = ap[1][0]; b1[1] = ap[1][1]; b1[2] = xw[1][0]; b1[3] = xw[1][1];
            X0 = MFMA(BF8(a4), BF8(b0), X0);
            X1 = MFMA(BF8(a4), BF8(b1), X1);
            if (gl < wb + 8) { if (wb != base) load_ap(base); break; }
          }
          u32x4 M[2][2];
#pragma unroll
          for (int s = 0; s < 2; s++)
#pragma unroll
            for (int j = 0; j < 4; j++) {
              M[0][s][j] = silu2(X0[8 * s + 2 * j], X0[8 * s + 2 * j + 1]);
              M[1][s][j] = silu2(X1[8 * s + 2 * j], X1[8 * s + 2 * j + 1]);
            }
          for (;;) {
            s16x4 sf[4];
            const u32 d0 = (u32)(Tb + 4 * hh + tq) - slo;
#pragma unroll
            for (int u = 0; u < 4; u++) sf[u] = trl(((d0 + 8u * u) < slen ? a_vr : a_z) + 64 * u);
#pragma unroll
            for (int s = 0; s < 2; s++) {
              const bf16x8 S = mk8(sf[2 * s], sf[2 * s + 1]);
              Y0 = MFMA(S, BF8(M[0][s]), Y0);
              Y1 = MFMA(S, BF8(M[1][s]), Y1);
            }
            if (gl < base + 8) break;
            flush(base); base += 8; load_ap(base); load_sel(base);
          }
        }
        flush(base);
        asm volatile("s_waitcnt vmcnt(0)" ::: "memory");
      }
    }
    STAMP(1, 7);
    if (ALLOW_FAST) {
      glds16(lds0 + L_GB + wv * 4096, (u32)(wv * 2048 + lane * 16), wh);
      glds16(lds0 + L_GB + wv * 4096 + 1024, (u32)(wv * 2048 + 1024 + lane * 16), wh);
    }
    if (lane == 0) misc[16 + slot] = (u32)head;
    BAR();
    {
      float* hb = (float*)(lds + L_HB) + wv * MS;
      const int hprev = slot > 0 ? (int)misc[16 + slot - 1] : 127, hnext = slot < 7 ? (int)misc[16 + slot + 1] : 127;
      const bool uniq = (hprev != head) && (hnext != head);
      if (head != 127) {
        const float v = hb[lane];
        hb[lane] = 0.f;
        if (uniq) macc[head * MS + lane] += v;
        else atomicAdd(&macc[head * MS + lane], v);
      }
      if (tid < 144) hist[tid] = 0;
    }
  };

  load_records(0, 4 * MAXR);
  STAMP(1, 2);
  int Pmax = 0, ntot = 0;
  {
    for (int i = tid; i < (99 + 8) * MS / 4; i += 512) *(f32x4*)(macc + 4 * i) = f32x4{0.f, 0.f, 0.f, 0.f};
    if (tid < 105) *(f32x4*)(pacc + 4 * tid) = f32x4{0.f, 0.f, 0.f, 0.f};
    if (tid < 64) *(u32*)(lds + L_ZERO + tid * 4) = 0;
    if (tid < 5) { u32x2 v; v[0] = tid == 0 ? 0x3F80u : (tid == 1 ? 0x3F800000u : 0u); v[1] = tid == 2 ? 0x3F80u : (tid == 3 ? 0x3F800000u : 0u); *(u32x2*)(lds + L_LUT + tid * 8) = v; }
#pragma unroll
    for (int i = 0; i < 2; i++) *(u32x4*)(lds + L_ST + (tid + 512 * i) * 16) = u32x4{0, 0, 0, 0};
    if (tid < 128) { f32x4 v; v[0] = bf2f(pd0); v[1] = bf2f(pd1); v[2] = bf2f(pd2); v[3] = 0.f; *(f32x4*)(lds + L_PD + tid * 16) = v; }
    if (tid < 144) hist[tid] = 0;
    if (lane == 0) { misc[wv] = (u32)P; misc[8 + wv] = (u32)csum; }
    u32x4 o;
#pragma unroll
    for (int i = 0; i < 4; i++) o[i] = pk_bf16(bflo(wB[i]) * NLOG2E, bfhi(wB[i]) * NLOG2E);
    const u32x4 z4 = {0, 0, 0, 0};
    *(u32x4*)(lds + L_WE + tid * 16) = wA;
    *(u32x4*)(lds + L_WE + 8192 + tid * 16) = o;
    if (tid < 40) *(u32x4*)(lds + L_WE + 16384 + tid * 16) = wC;
    *(u32x4*)(lds + L_H2 + tid * 16) = tid < nn * 8 ? hA : z4;
    if (tid < 288) *(u32x4*)(lds + L_H2 + 8192 + tid * 16) = tid + 512 < nn * 8 ? hB : z4;
    BAR();
    STAMP(1, 3);
#pragma unroll
    for (int i = 0; i < 8; i++) { Pmax = max(Pmax, (int)misc[i]); ntot += (int)misc[8 + i]; }
    Pmax = __builtin_amdgcn_readfirstlane(Pmax); ntot = __builtin_amdgcn_readfirstlane(ntot);
#pragma unroll
    for (int s = 0; s < 4; s++)
#pragma unroll
      for (int ct = 0; ct < 2; ct++) {
        const u32 p = lds0 + (u32)(L_WE + (64 + 32 * hh + 8 * s + tq) * 128 + (32 * ct + 16 * blk + 4 * tp) * 2);
        Bw[s][ct] = mk8(trl(p), trl(p + 4 * 128));
      }
#pragma unroll
    for (int ct = 0; ct < 2; ct++) {
      const u16* wt = (const u16*)(lds + L_WE + 16384);
      const int col = 32 * ct + r;
      const float w0 = bf2f(wt[col]) * NLOG2E, w1 = bf2f(wt[64 + col]) * NLOG2E, w2 = bf2f(wt[128 + col]) * NLOG2E,
                  w3 = bf2f(wt[192 + col]) * NLOG2E, w4 = bf2f(wt[256 + col]) * NLOG2E;
      const u32 hi = pk_bf16(w0, 0.f) & 0xffffu;
      const u32 lo = pk_bf16(w0 - bf2f(hi), 0.f) & 0xffffu;
      xw[ct][0] = hh ? hi : pk_bf16(w1, w2);
      xw[ct][1] = hh ? lo : pk_bf16(w3, w4);
    }
    {
      const int nt = wv >> 1, ct = wv & 1;
      f32x16 acc = {};
#pragma unroll
      for (int s = 0; s < 4; s++) {
        const u32 p = lds0 + (u32)(L_WE + (32 * hh + 8 * s + tq) * 128 + (32 * ct + 16 * blk + 4 * tp) * 2);
        bf16x8 aw = mk8(trl(p), trl(p + 4 * 128));
        bf16x8 bh = *(const bf16x8*)(lds + L_H2 + min(32 * nt + r, 99) * 128 + (4 * hh + s) * 16);
        acc = MFMA(aw, bh, acc);
      }
      const int node = 32 * nt + r;
      char* ap = lds + (node < 105 ? L_AP + node * 128 : L_DESC + 2048 + wv * 128) + (32 * ct + 4 * hh) * 2;
#pragma unroll
      for (int q = 0; q < 4; q++) {
        u32x2 v;
        v[0] = pk_bf16(acc[4 * q] * NLOG2E, acc[4 * q + 1] * NLOG2E);
        v[1] = pk_bf16(acc[4 * q + 2] * NLOG2E, acc[4 * q + 3] * NLOG2E);
        *(u32x2*)(ap + 16 * q) = v;
      }
    }
  }
  if (Pmax <= 4 * MAXR && ntot <= CAP) {
    process(std::integral_constant<bool, true>{});
  } else {
    for (int plo = 0; plo < Pmax; plo += FPPB) {
      BAR();
      load_records(plo, FPPB);
      process(std::integral_constant<bool, false>{});
    }
  }
  STAMP(1, 8);

  if (!(Pmax <= 4 * MAXR && ntot <= CAP)) {
    glds16(lds0 + L_GB + wv * 4096, (u32)(wv * 2048 + lane * 16), wh);
    glds16(lds0 + L_GB + wv * 4096 + 1024, (u32)(wv * 2048 + 1024 + lane * 16), wh);
  }
  asm volatile("s_waitcnt vmcnt(0)" ::: "memory");
  BAR();
  {
    int ln = lane;
    asm volatile("" : "+v"(ln));
    const int r = ln & 31, hh = ln >> 5;
    const int G4 = ln >> 4, i16 = ln & 15, tq = i16 >> 2, tp = i16 & 3, blk = G4 & 1;
    const int nt = wv >> 1, ct = wv & 1;
    const int node = 32 * nt + r;
    const int nrow = node < NB ? node : MDUMMY;
    f32x16 acc = {};
#pragma unroll
    for (int s = 0; s < 8; s++) {
      const int kb = (s < 4) ? (32 * hh + 8 * s) : (64 + 32 * hh + 8 * (s - 4));
      const int k0 = kb + tq;
      const u32 p = lds0 + (u32)(L_GB + (k0 >> 4) * 4096 + (k0 & 15) * 128 + (32 * ct + 16 * blk + 4 * tp) * 2);
      const bf16x8 aw = mk8(trl(p), trl(p + 4 * 128));
      bf16x8 bb;
      if (s < 4) {
        bb = *(const bf16x8*)(lds + L_H2 + min(node, 99) * 128 + (4 * hh + s) * 16);
      } else {
        const float* mp = macc + nrow * MS + 32 * hh + 8 * (s - 4);
        const f32x4 m0 = *(const f32x4*)mp, m1 = *(const f32x4*)(mp + 4);
        u32x4 pk;
        pk[0] = pk_bf16(m0[0] * NLN2, m0[1] * NLN2); pk[1] = pk_bf16(m0[2] * NLN2, m0[3] * NLN2);
        pk[2] = pk_bf16(m1[0] * NLN2, m1[1] * NLN2); pk[3] = pk_bf16(m1[2] * NLN2, m1[3] * NLN2);
        bb = __builtin_bit_cast(bf16x8, pk);
      }
      acc = MFMA(aw, bb, acc);
    }
#pragma unroll
    for (int q = 0; q < 4; q++) {
      float u[4];
#pragma unroll
      for (int i = 0; i < 4; i++) {
        const float x = bf2f(pk_bf16(acc[4 * q + i], 0.f) & 0xffffu);
        u[i] = x * __builtin_amdgcn_rcpf(1.0f + __builtin_amdgcn_exp2f(x * NLOG2E));
      }
      u32x2 v; v[0] = pk_bf16(u[0], u[1]); v[1] = pk_bf16(u[2], u[3]);
      *(u32x2*)(lds + L_UT + node * 144 + (32 * ct + 8 * q + 4 * hh) * 2) = v;
    }
  }
  BAR();
#pragma unroll
  for (int k = 0; k < 2; k++) {
    const int i = tid + 512 * k;
    if (i < nn * 8) {
      const int row = i >> 3, chn = i & 7;
      const u32x4 u = *(const u32x4*)(lds + L_UT + row * 144 + chn * 16);
      const u32x4 hv = *(const u32x4*)(lds + L_H2 + i * 16);
      u32x4 o;
#pragma unroll
      for (int j = 0; j < 4; j++) o[j] = pk_bf16(bflo(hv[j]) + bflo(u[j]), bfhi(hv[j]) + bfhi(u[j]));
      *(u32x4*)(out + (size_t)(node0 + row) * 64 + chn * 8) = o;
    }
  }
  if (tid < nn * 3) {
    const int nd = tid / 3, c = tid - nd * 3;
    const float pv = *(const float*)(lds + L_PD + nd * 16 + c * 4);
    const float dlt = bf2f(pk_bf16(pacc[nd * 4 + c], 0.f) & 0xffffu);
    out[(size_t)N * 64 + (size_t)node0 * 3 + tid] = (u16)(pk_bf16(pv + dlt, 0.f) & 0xffffu);
  }
  STAMP(1, 9);
  STAMP_OUT(1, R);
}

extern "C" void kernel_launch(void* const* d_in, const int* in_sizes, int n_in, void* d_out, int out_size, void* d_ws,
                              size_t ws_size, hipStream_t stream) {
  const u16* h = (const u16*)d_in[0];
  const u16* pos = (const u16*)d_in[1];
  const u16* ea = (const u16*)d_in[2];
  const int* src = (const int*)d_in[3];
  const int* dst = (const int*)d_in[4];
  const u16* we = (const u16*)d_in[5];
  const u16* wx = (const u16*)d_in[6];
  const u16* wh = (const u16*)d_in[7];
  const int N = in_sizes[0] / 64, E = in_sizes[3];
  char* ws = (char*)d_ws;
  u32* R = (u32*)ws;
  f32x4* P4 = (f32x4*)(ws + (size_t)10 * 1048576);
  u32* T = (u32*)(ws + (size_t)11 * 1048576);
  (void)hipFuncSetAttribute((const void*)k1_partition, hipFuncAttributeMaxDynamicSharedMemorySize, K1_LDS);
  (void)hipFuncSetAttribute((const void*)k2_main, hipFuncAttributeMaxDynamicSharedMemorySize, K2_LDS);
  k1_partition<<<(E + CH - 1) / CH, 1024, K1_LDS, stream>>>((const u32x4*)h, pos, (const u32x4*)ea, (const i32x4*)src, (const i32x4*)dst, R, P4, T, N, E);
  k2_main<<<(N + NB - 1) / NB, 512, K2_LDS, stream>>>(h, pos, we, wx, wh, R, P4, T, (u16*)d_out, N);
}
```

```cpp
#include <hip/hip_runtime.h>
#include <stdint.h>
#include <type_traits>

typedef unsigned short u16;
typedef unsigned char u8;
typedef unsigned int u32;
typedef short s16x4 __attribute__((ext_vector_type(4)));
typedef __bf16 bf16x2 __attribute__((ext_vector_type(2)));
typedef __bf16 bf16x8 __attribute__((ext_vector_type(8)));
typedef float f32x2 __attribute__((ext_vector_type(2)));
typedef float f32x4 __attribute__((ext_vector_type(4)));
typedef float f32x16 __attribute__((ext_vector_type(16)));
typedef u32 u32x2 __attribute__((ext_vector_type(2)));
typedef u32 u32x3 __attribute__((ext_vector_type(3)));
typedef u32 u32x4 __attribute__((ext_vector_type(4)));
typedef int i32x4 __attribute__((ext_vector_type(4)));
#define LAS __attribute__((address_space(3)))
#define DI __device__ __forceinline__
#define AI __attribute__((always_inline))

constexpr int NB    = 98;
constexpr int CH    = 3128;
constexpr int NCH   = 256;
constexpr int CAP   = 3392;
constexpr int MAXR  = 12;
constexpr int FPPB  = 26;
#define SPLIT 86
constexpr float NLOG2E = -1.4426950408889634f;
constexpr float NLN2   = -0.6931471805599453f;

DI float bf2f(u32 bits16) { return __uint_as_float(bits16 << 16); }
DI float bflo(u32 v) { return __uint_as_float(v << 16); }
DI float bfhi(u32 v) { return __uint_as_float(v & 0xffff0000u); }
DI u32 pk_bf16(float lo, float hi) {
  f32x2 v; v[0] = lo; v[1] = hi;
  return __builtin_bit_cast(u32, __builtin_convertvector(v, bf16x2));
}
template <int CTRL, int RMASK> DI int dpp_i(int x) { return __builtin_amdgcn_update_dpp(0, x, CTRL, RMASK, 0xf, false); }
template <int CTRL> DI float dpp_f(float x) {
  return __builtin_bit_cast(float, __builtin_amdgcn_update_dpp(0, __builtin_bit_cast(int, x), CTRL, 0xf, 0xf, true));
}
DI int wave_incl_scan(int x) {
  x += dpp_i<0x111, 0xf>(x); x += dpp_i<0x112, 0xf>(x); x += dpp_i<0x114, 0xf>(x); x += dpp_i<0x118, 0xf>(x);
  x += dpp_i<0x142, 0xa>(x); x += dpp_i<0x143, 0xc>(x);
  return x;
}
DI float half_reduce(float x) {
  x += dpp_f<0x111>(x); x += dpp_f<0x112>(x); x += dpp_f<0x114>(x); x += dpp_f<0x118>(x);
  x += dpp_f<0x142>(x);
  return x;
}
DI void glds16(u32 lds_addr, u32 voff, const void* base) {
  asm volatile("s_mov_b32 m0, %0\n\ts_nop 0\n\tglobal_load_lds_dwordx4 %1, %2" ::"s"(lds_addr), "v"(voff), "s"(base) : "memory");
}
DI void glds16_lo32(u32 lds_addr, u32 voff, const void* base) {
  asm volatile("s_mov_b32 m0, %0\n\ts_mov_b64 exec, 0xffffffff\n\tglobal_load_lds_dwordx4 %1, %2\n\ts_mov_b64 exec, -1"
               ::"s"(lds_addr), "v"(voff), "s"(base) : "memory");
}
DI void glds_idx(u32 lds_addr, u32 idx, u32 mul, u32 add, const void* base) {
  u32 t;
  asm volatile("s_mov_b32 m0, %1\n\tv_mad_u32_u16 %0, %2, %3, %4\n\tglobal_load_lds_dwordx4 %0, %5"
               : "=&v"(t) : "s"(lds_addr), "v"(idx), "s"(mul), "v"(add), "s"(base) : "memory");
}
DI void glds_idx_lo32(u32 lds_addr, u32 idx, const void* base) {
  u32 t;
  asm volatile("s_mov_b32 m0, %1\n\tv_mad_u32_u16 %0, %2, 16, 0\n\ts_mov_b64 exec, 0xffffffff\n\tglobal_load_lds_dwordx4 %0, %3\n\ts_mov_b64 exec, -1"
               : "=&v"(t) : "s"(lds_addr), "v"(idx), "s"(base) : "memory");
}
DI u32 mad16(u32 a_lo16, u32 b, u32 c) {
  u32 d;
  asm("v_mad_u32_u16 %0, %1, %2, %3" : "=v"(d) : "v"(a_lo16), "s"(b), "v"(c));
  return d;
}
template <int HALF> DI void half_write_b64(u32& prev, u32 a, u32x2 v) {
  if constexpr (HALF == 0)
    asm volatile("s_mov_b64 exec, 0xffffffff\n\tds_write_b64 %1, %2\n\tv_mov_b32 %0, %1\n\ts_mov_b64 exec, -1" : "+v"(prev) : "v"(a), "v"(v) : "memory");
  else
    asm volatile("s_mov_b64 exec, %3\n\tds_write_b64 %1, %2\n\tv_mov_b32 %0, %1\n\ts_mov_b64 exec, -1" : "+v"(prev) : "v"(a), "v"(v), "s"(0xffffffff00000000ull) : "memory");
}
DI void reduce12(float (&v)[12], float& x0, float& x1) {
  asm volatile(
      "s_nop 1\n\t"
      "v_permlane16_swap_b32 %0, %1\n\tv_permlane16_swap_b32 %2, %3\n\tv_permlane16_swap_b32 %4, %5\n\t"
      "v_permlane16_swap_b32 %6, %7\n\tv_permlane16_swap_b32 %8, %9\n\tv_permlane16_swap_b32 %10, %11\n\t"
      "s_nop 1\n\t"
      "v_add_f32 %0, %0, %1\n\tv_add_f32 %2, %2, %3\n\tv_add_f32 %4, %4, %5\n\t"
      "v_add_f32 %6, %6, %7\n\tv_add_f32 %8, %8, %9\n\tv_add_f32 %10, %10, %11\n\t"
      "v_add_f32_dpp %1, %0, %0 row_ror:8 row_mask:0xf bank_mask:0x3\n\t"
      "v_add_f32_dpp %3, %4, %4 row_ror:8 row_mask:0xf bank_mask:0x3\n\t"
      "v_add_f32_dpp %5, %8, %8 row_ror:8 row_mask:0xf bank_mask:0x3\n\t"
      "v_add_f32_dpp %1, %2, %2 row_ror:8 row_mask:0xf bank_mask:0xc\n\t"
      "v_add_f32_dpp %3, %6, %6 row_ror:8 row_mask:0xf bank_mask:0xc\n\t"
      "v_add_f32_dpp %5, %10, %10 row_ror:8 row_mask:0xf bank_mask:0xc\n\t"
      "v_add_f32_dpp %7, %1, %1 row_half_mirror row_mask:0xf bank_mask:0x5\n\t"
      "v_add_f32_dpp %7, %3, %3 row_half_mirror row_mask:0xf bank_mask:0xa\n\t"
      "v_add_f32_dpp %9, %5, %5 row_half_mirror row_mask:0xf bank_mask:0xf\n\t"
      "s_nop 1\n\t"
      "v_add_f32_dpp %7, %7, %7 quad_perm:[1,0,3,2] row_mask:0xf bank_mask:0xf\n\t"
      "v_add_f32_dpp %9, %9, %9 quad_perm:[1,0,3,2] row_mask:0xf bank_mask:0xf\n\t"
      "s_nop 1\n\t"
      "v_add_f32_dpp %7, %7, %7 quad_perm:[2,3,0,1] row_mask:0xf bank_mask:0xf\n\t"
      "v_add_f32_dpp %9, %9, %9 quad_perm:[2,3,0,1] row_mask:0xf bank_mask:0xf\n\t"
      "s_nop 0"
      : "+v"(v[0]), "+v"(v[1]), "+v"(v[2]), "+v"(v[3]), "+v"(v[4]), "+v"(v[5]), "+v"(v[6]), "+v"(v[7]), "+v"(v[8]), "+v"(v[9]),
        "+v"(v[10]), "+v"(v[11]));
  x0 = v[7]; x1 = v[9];
}
DI void masked_add2(u32 a0, float x0, u32 a1, float x1) {
  asm volatile("s_mov_b64 exec, %4\n\tds_add_f32 %0, %1\n\ts_mov_b64 exec, %5\n\tds_add_f32 %2, %3\n\ts_mov_b64 exec, -1"
               ::"v"(a0), "v"(x0), "v"(a1), "v"(x1), "s"(0x1111111111111111ull), "s"(0x0101010101010101ull) : "memory");
}
template <class T> DI T ldl(u32 a) { return *(const LAS T*)(uintptr_t)a; }
template <class T> DI void stl(u32 a, T v) { *(LAS T*)(uintptr_t)a = v; }
DI s16x4 trl(u32 a) { return __builtin_amdgcn_ds_read_tr16_b64_v4i16((LAS s16x4*)(uintptr_t)a); }
DI bf16x8 mk8(s16x4 lo, s16x4 hi) {
  return __builtin_bit_cast(bf16x8, __builtin_shufflevector(lo, hi, 0, 1, 2, 3, 4, 5, 6, 7));
}
#define BAR() do { asm volatile("s_waitcnt lgkmcnt(0)" ::: "memory"); __builtin_amdgcn_s_barrier(); asm volatile("" ::: "memory"); } while (0)
#define SB() __builtin_amdgcn_sched_barrier(0)
#define PIN(x) asm volatile("" : "+v"(x))
template <int I, int N, class F> DI void static_for(F&& f) {
  if constexpr (I < N) { f(std::integral_constant<int, I>{}); static_for<I + 1, N>(f); }
}
template <int I> using IC = std::integral_constant<int, I>;
#define MFMA(a, b, c) __builtin_amdgcn_mfma_f32_32x32x16_bf16((a), (b), (c), 0, 0, 0)
#define BF8(x) __builtin_bit_cast(bf16x8, (x))

constexpr int K1_HIST = CH * 12 + 64;
constexpr int K1_OFF = K1_HIST + 1024 + 64;
constexpr int K1_LDS = K1_OFF + 1024 + 64;

__global__ __launch_bounds__(1024) void k1_partition(const u16* __restrict__ pos, const u32x4* __restrict__ ea4,
                                                     const i32x4* __restrict__ src4, const i32x4* __restrict__ dst4,
                                                     u32* __restrict__ R, f32x4* __restrict__ P4, u32* __restrict__ T,
                                                     int N, int E) {
  extern __shared__ __attribute__((aligned(16))) char lds[];
  const int tid = threadIdx.x, lane = tid & 63;
  const int wv = __builtin_amdgcn_readfirstlane(tid >> 6);
  const int w = blockIdx.x;
  u32* hist = (u32*)(lds + K1_HIST);
  u32* off = (u32*)(lds + K1_OFF);
  int cnt = E - w * CH; cnt = cnt > CH ? CH : cnt; cnt = cnt < 4 ? 4 : cnt;
  const bool act = 4 * tid < cnt;
  const int q = (w * CH >> 2) + (act ? tid : 0);
  const i32x4 d4 = __builtin_nontemporal_load(&dst4[q]);
  const i32x4 s4 = __builtin_nontemporal_load(&src4[q]);
  const u32x4 a0 = __builtin_nontemporal_load(&ea4[2 * q]);
  const u32x4 a1 = __builtin_nontemporal_load(&ea4[2 * q + 1]);
  {
    const int n = w * NB + (tid - 896);
    if (tid >= 896 && tid < 896 + NB && n < N) {
      const u16* p = pos + (size_t)n * 3;
      f32x4 v; v[0] = bf2f(p[0]); v[1] = bf2f(p[1]); v[2] = bf2f(p[2]); v[3] = 0.f;
      P4[n] = v;
    }
  }
  if (tid < 272) hist[tid] = 0;
  BAR();
  int bk[4], gl[4], rk[4];
#pragma unroll
  for (int i = 0; i < 4; i++) {
    const u32 d = (u32)d4[i];
    bk[i] = (int)((d * 42800u) >> 22);
    gl[i] = (int)d - bk[i] * NB;
    bk[i] = act ? bk[i] : 256 + (lane & 15);
    rk[i] = atomicAdd(&hist[bk[i]], 1u);
  }
  BAR();
  {
    u32x4 c = *(const u32x4*)(hist + 4 * lane);
    int s = c[0] + c[1] + c[2] + c[3];
    int inc = wave_incl_scan(s);
    u32x4 o;
    o[0] = inc - s; o[1] = o[0] + c[0]; o[2] = o[1] + c[1]; o[3] = o[2] + c[2];
    *(u32x4*)(off + 4 * lane) = o;
    if (wv == 15) {
#pragma unroll
      for (int i = 0; i < 4; i++) T[(4 * lane + i) * NCH + w] = o[i] | (c[i] << 16);
    }
  }
  if (act) {
#pragma unroll
    for (int i = 0; i < 4; i++) {
      const int slot = (int)(off[bk[i]] + rk[i]);
      u32* p = (u32*)(lds + slot * 12);
      p[0] = i < 2 ? a0[2 * i] : a1[2 * i - 4];
      p[1] = i < 2 ? a0[2 * i + 1] : a1[2 * i - 3];
      p[2] = (u32)s4[i] | ((u32)gl[i] << 16);
    }
  }
  BAR();
  u32x4* Rw = (u32x4*)(R + (size_t)w * CH * 3);
  const int n16 = (cnt * 12 + 15) >> 4;
#pragma unroll
  for (int k = 0; k < 3; k++) {
    const int i = tid + 1024 * k;
    if (i < n16) __builtin_nontemporal_store(*(const u32x4*)(lds + i * 16), &Rw[i]);
  }
}

constexpr int L_GB = 0;
constexpr int L_PB = L_GB + 8 * 4096;
constexpr int L_D2 = L_PB + 8 * 1024;
constexpr int L_ST = L_D2 + 8 * 512;
constexpr int L_EA = L_ST + 8 * 2048;
constexpr int L_SG = L_EA + (CAP + 32) * 8;
constexpr int SGPAD = 128;
constexpr int L_AP = L_SG + (CAP + 160) * 4;
constexpr int MS = 68;
constexpr int MDUMMY = 98;
constexpr int L_MACC = L_AP + 105 * 128;
constexpr int L_HB = L_MACC + 99 * MS * 4;
constexpr int L_PACC = L_HB + 8 * MS * 4;
constexpr int L_HIST = L_PACC + 105 * 16;
constexpr int L_OFF = L_HIST + 144 * 4;
constexpr int L_ZERO = L_OFF + 144 * 4;
constexpr int L_LUT = L_ZERO + 256;
constexpr int L_PD = L_LUT + 64;
constexpr int L_MISC = L_PD + 128 * 16;
constexpr int L_H2 = L_MISC + 256;
constexpr int K2_LDS = L_H2 + 12800;
static_assert(K2_LDS <= 163840, "LDS");
static_assert(L_AP % 16 == 0 && L_MACC % 16 == 0 && L_EA % 16 == 0 && L_SG % 16 == 0 && L_HB % 16 == 0 && L_PD % 16 == 0 && L_H2 % 16 == 0, "align");
constexpr int L_WE = L_EA;
constexpr int L_DESC = L_GB;
constexpr int L_UT = L_EA;
constexpr int L_WH = L_EA + 128 * 144;

DI u32 silu2(float z0, float z1) {
  f32x2 z; z[0] = z0; z[1] = z1;
  f32x2 t; t[0] = __builtin_amdgcn_exp2f(z0); t[1] = __builtin_amdgcn_exp2f(z1);
  t = t + 1.0f;
  f32x2 r; r[0] = __builtin_amdgcn_rcpf(t[0]); r[1] = __builtin_amdgcn_rcpf(t[1]);
  f32x2 m = z * r;
  return __builtin_bit_cast(u32, __builtin_convertvector(m, bf16x2));
}

__global__ __launch_bounds__(512) void k2_main(const u16* __restrict__ h, const u16* __restrict__ pos,
                                               const u16* __restrict__ we, const u16* __restrict__ wx,
                                               const u16* __restrict__ wh, const u32* __restrict__ R,
                                               const f32x4* __restrict__ P4, const u32* __restrict__ T,
                                               u16* __restrict__ out, int N) {
  extern __shared__ __attribute__((aligned(16))) char lds[];
  const int tid = threadIdx.x, lane = tid & 63;
  const int wv = __builtin_amdgcn_readfirstlane(tid >> 6);
  const int b = blockIdx.x;
  const int node0 = b * NB;
  int nn = N - node0; nn = nn > NB ? NB : nn; nn = nn < 1 ? 1 : nn;
  const int r = lane & 31, hh = lane >> 5;
  const u32 lds0 = (u32)(uintptr_t)(LAS char*)lds;
  u32* hist = (u32*)(lds + L_HIST);
  u32* off = (u32*)(lds + L_OFF);
  float* macc = (float*)(lds + L_MACC);
  float* pacc = (float*)(lds + L_PACC);
  u32* misc = (u32*)(lds + L_MISC);

  const u32 tv = T[b * NCH + wv * 32 + r];
  const u32x4* we16 = (const u32x4*)we;
  const u32x4 wA = we16[tid], wB = we16[512 + tid];
  const u32x4 wC = we16[1024 + (tid < 40 ? tid : 0)];
  const u32x4* h16 = (const u32x4*)(h + (size_t)node0 * 64);
  const u32x4 hA = h16[tid < nn * 8 ? tid : 0];
  const u32x4 hB = h16[tid + 512 < nn * 8 ? tid + 512 : 0];
  const u32x4* wh16 = (const u32x4*)wh;
  const u16* pp = pos + (size_t)(node0 + (tid < nn ? tid : 0)) * 3;
  const u32 pd0 = pp[0], pd1 = pp[1], pd2 = pp[2];
  const float wx0 = bf2f(wx[r]) * NLN2, wx1 = bf2f(wx[32 + r]) * NLN2;

  const u32 cstart = tv & 0xffffu;
  const u32 ccnt = lane < 32 ? (tv >> 16) : 0u;
  const int np = (int)((ccnt + 15) >> 4);
  const int pinc = wave_incl_scan(np);
  const int pexc = pinc - np;
  const int P = __builtin_amdgcn_readlane(pinc, 31);
  const int csum = __builtin_amdgcn_readlane(wave_incl_scan((int)ccnt), 31);

  const int G4 = lane >> 4, i16 = lane & 15, tq = i16 >> 2, tp = i16 & 3, blk = G4 & 1;
  bf16x8 Bw[4][2];
  u32 xw[2][2];
  u32x3 rec[MAXR];
  u32 vmask = 0;
  int nrounds = 0;
#pragma unroll
  for (int rr = 8; rr < MAXR; rr++) rec[rr] = u32x3{0, 0, 0};
  u32* desc = (u32*)(lds + L_DESC + wv * 256);
  auto load_records = [&](int plo, int ppb) AI {
    int k0 = plo - pexc; k0 = k0 < 0 ? 0 : k0;
    int k1 = plo + ppb - pexc; k1 = k1 > np ? np : k1;
    const u32 gbase = (u32)((wv * 32 + r) * CH) + cstart;
    for (int k = k0; k < k1; k++) {
      u32 nrec = ccnt - 16 * k; nrec = nrec > 16 ? 16 : nrec;
      desc[pexc + k - plo] = (gbase + 16 * k) | (nrec << 24);
    }
    int Pb = P - plo; Pb = Pb < 0 ? 0 : Pb; Pb = Pb > ppb ? ppb : Pb;
    nrounds = (Pb + 3) >> 2;
    vmask = 0;
    auto rounds = [&](auto lo_c, auto hi_c) AI {
      constexpr int lo = decltype(lo_c)::value, hi = decltype(hi_c)::value;
      u32 dd[hi - lo];
#pragma unroll
      for (int rr = lo; rr < hi; rr++) dd[rr - lo] = desc[4 * rr + G4];
#pragma unroll
      for (int rr = lo; rr < hi; rr++) {
        const u32 d = (4 * rr + G4 < Pb) ? dd[rr - lo] : 0u;
        const bool v = (u32)i16 < (d >> 24);
        const u32 gi = (d & 0xffffffu) + (v ? i16 : 0);
        rec[rr] = __builtin_nontemporal_load((const u32x3*)(R + (size_t)gi * 3));
        vmask |= v ? (1u << rr) : 0u;
      }
    };
    rounds(IC<0>{}, IC<8>{});
    if (nrounds > 8) rounds(IC<8>{}, IC<10>{});
    if (nrounds > 10) rounds(IC<10>{}, IC<12>{});
  };
  auto process = [&](auto fast_tag) AI {
    constexpr bool ALLOW_FAST = decltype(fast_tag)::value;
    u32 rk[MAXR];
    auto ranks = [&](auto lo_c, auto hi_c) AI {
      constexpr int lo = decltype(lo_c)::value, hi = decltype(hi_c)::value;
#pragma unroll
      for (int rr = lo; rr < hi; rr++) {
        const u32 g = (vmask >> rr) & 1u ? (rec[rr][2] >> 16) : 128u + (lane & 15);
        rk[rr] = atomicAdd(&hist[g], 1u);
      }
    };
#pragma unroll
    for (int rr = 8; rr < MAXR; rr++) rk[rr] = 0;
    ranks(IC<0>{}, IC<8>{});
    if (nrounds > 8) ranks(IC<8>{}, IC<10>{});
    if (nrounds > 10) ranks(IC<10>{}, IC<12>{});
    BAR();
    int n, glast;
    {
      u32x2 c = *(const u32x2*)(hist + 2 * lane);
      int s = c[0] + c[1];
      int inc = wave_incl_scan(s);
      u32x2 o; o[0] = inc - s; o[1] = o[0] + c[0];
      *(u32x2*)(off + 2 * lane) = o;
      n = __builtin_amdgcn_readlane(inc, 63);
      if (lane < 16) off[128 + lane] = n;
      const unsigned long long m1 = __ballot(c[1] != 0), m0 = __ballot(c[0] != 0);
      const int l1 = m1 ? 63 - __builtin_clzll(m1) : -1, l0 = m0 ? 63 - __builtin_clzll(m0) : -1;
      glast = max(2 * l1 + 1, 2 * l0); glast = glast < 0 ? 0 : glast;
    }
    const int ntiles = (n + 31) >> 5;
    auto scatter = [&](auto lo_c, auto hi_c) AI {
      constexpr int lo = decltype(lo_c)::value, hi = decltype(hi_c)::value;
      u32 ps[hi - lo];
#pragma unroll
      for (int rr = lo; rr < hi; rr++) {
        const bool v = (vmask >> rr) & 1u;
        const u32 g = v ? (rec[rr][2] >> 16) : 128u;
        ps[rr - lo] = off[g] + rk[rr];
      }
#pragma unroll
      for (int rr = lo; rr < hi; rr++) {
        const bool v = (vmask >> rr) & 1u;
        const u32 p = v ? ps[rr - lo] : (u32)(CAP + (lane & 31));
        u32x2 e; e[0] = rec[rr][0]; e[1] = rec[rr][1];
        *(u32x2*)(lds + L_EA + p * 8) = e;
        *(u32*)(lds + L_SG + (v ? p : p + (u32)SGPAD) * 4) = rec[rr][2];
      }
    };
    scatter(IC<0>{}, IC<8>{});
    if (nrounds > 8) scatter(IC<8>{}, IC<10>{});
    if (nrounds > 10) scatter(IC<10>{}, IC<12>{});
    {
      const int ps = n + lane + 64 * (wv & 3);
      if (wv >= 4 && ps < ntiles * 32 + SGPAD) {
        u32x2 z; z[0] = 0; z[1] = 0;
        if (ps < ntiles * 32) *(u32x2*)(lds + L_EA + ps * 8) = z;
        *(u32*)(lds + L_SG + ps * 4) = 127u << 16;
      }
    }
    BAR();

    int ln = lane;
    asm volatile("" : "+v"(ln));
    const int r = ln & 31, hh = ln >> 5;
    const int G4 = ln >> 4, i16 = ln & 15, tq = i16 >> 2, tp = i16 & 3, blk = G4 & 1;
    const int kk = ln >> 3, c8 = ln & 7;
    u32 gcst[4], aoff[4];
#pragma unroll
    for (int i = 0; i < 4; i++) gcst[i] = (u32)((c8 ^ i ^ (((kk >> 1) & 1) << 2)) * 16);
    {
      const int ii = r & 3, k2 = r >> 2;
#pragma unroll
      for (int s = 0; s < 4; s++)
        aoff[s] = lds0 + (u32)(L_GB + wv * 4096 + (ii * 8 + k2) * 128 + (((4 * hh + s) ^ ii ^ (((k2 >> 1) & 1) << 2)) * 16));
    }
    const int slot = (wv & 3) * 2 + (wv >> 2);
    auto cum = [&](int k) { return (ntiles * ((k >> 1) * 128 + (k & 1) * SPLIT)) >> 9; };
    const int t0 = cum(slot), t1 = cum(slot + 1);
    int head = 127;
    if (t0 < t1) {
      const u32 gb = lds0 + L_GB + wv * 4096;
      const u32 pb = lds0 + L_PB + wv * 1024;
      const u32 d2b = lds0 + L_D2 + wv * 512;
      const u32 a_vw = d2b + r * 8;
      const u32 a_vr = d2b + (4 * hh + tq) * 8;
      const u32 a_z = lds0 + L_ZERO;
      auto sk_read = [&](u32 a_k, u32x4& sk) AI {
        sk[0] = ldl<u32>(a_k); sk[1] = ldl<u32>(a_k + 4); sk[2] = ldl<u32>(a_k + 8); sk[3] = ldl<u32>(a_k + 12);
      };
      auto issue_h = [&](const u32x4& sk) AI {
        glds_idx(gb, sk[0], 128u, gcst[0], h);
        glds_idx(gb + 1024, sk[1], 128u, gcst[1], h);
        glds_idx(gb + 2048, sk[2], 128u, gcst[2], h);
        glds_idx(gb + 3072, sk[3], 128u, gcst[3], h);
      };
      u32 a_sk = lds0 + (u32)(L_SG + (t0 * 32 + 4 * kk) * 4), a_sr = lds0 + (u32)(L_SG + (t0 * 32 + r) * 4);
      { u32x4 sk; sk_read(a_sk, sk); issue_h(sk); a_sk += 128; }
      const int tl = (t0 + ln < t1) ? t0 + ln : t1 - 1;
      const int gfv = (int)(*(const u32*)(lds + L_SG + tl * 128) >> 16);
      const int glv = min((int)(*(const u32*)(lds + L_SG + tl * 128 + 124) >> 16), glast);
      const bool wide = __ballot(glv - gfv >= 8) != 0ull || (t1 - t0) > 64;
      int base = __builtin_amdgcn_readlane(gfv, 0);
      if ((int)__builtin_amdgcn_readfirstlane(off[base]) < t0 * 32) head = base;
      const int gspan = __builtin_amdgcn_readlane(glv, 63);
      f32x16 Y0 = {}, Y1 = {};
      u32 ap[2][2];
      auto load_ap = [&](int bs) AI {
#pragma unroll
        for (int ct = 0; ct < 2; ct++) {
          s16x4 v = trl(lds0 + (u32)(L_AP + (bs + 4 * hh + tq) * 128 + (32 * ct + 16 * blk + 4 * tp) * 2));
          u32x2 u = __builtin_bit_cast(u32x2, v);
          ap[ct][0] = u[0]; ap[ct][1] = u[1];
        }
      };
      u32 pa0, pa1;
      {
        const int b4 = (ln >> 4) & 1, b3 = (ln >> 3) & 1, b2 = (ln >> 2) & 1;
        const int j0 = 4 * b2 + 2 * b3 + b4, j1 = 8 + 2 * b3 + b4;
        pa0 = lds0 + (u32)(L_PACC + ((2 * (j0 / 3) + hh) * 4 + (j0 % 3)) * 4);
        pa1 = lds0 + (u32)(L_PACC + ((2 * (j1 / 3) + hh) * 4 + (j1 % 3)) * 4);
      }
      auto flush = [&](int bs) AI {
        u32 ma[4];
        float o0[4], o1[4];
        const u32 mrow = lds0 + (u32)(L_MACC + hh * (MS * 4) + 4 * r) + (u32)(bs * (MS * 4));
        if (bs + 7 <= gspan && (head == 127 || bs > head)) {
#pragma unroll
          for (int q = 0; q < 4; q++) ma[q] = mrow + (u32)(2 * q * (MS * 4));
        } else {
#pragma unroll
          for (int q = 0; q < 4; q++) {
            const int nd = bs + 2 * q + hh;
            u32 a = mrow + (u32)(2 * q * (MS * 4));
            a = nd > gspan ? lds0 + (u32)(L_MACC + MDUMMY * (MS * 4)) + 4 * r : a;
            a = nd == head ? lds0 + (u32)(L_HB + wv * (MS * 4)) + 4 * r : a;
            ma[q] = a;
          }
        }
#pragma unroll
        for (int q = 0; q < 4; q++) { o0[q] = ldl<float>(ma[q]); o1[q] = ldl<float>(ma[q] + 128); }
        float pr[12];
#pragma unroll
        for (int q = 0; q < 4; q++)
#pragma unroll
          for (int c = 1; c < 4; c++) pr[q * 3 + c - 1] = Y0[4 * q + c] * wx0 + Y1[4 * q + c] * wx1;
        SB();
#pragma unroll
        for (int q = 0; q < 4; q++) { stl<float>(ma[q], o0[q] + Y0[4 * q]); stl<float>(ma[q] + 128, o1[q] + Y1[4 * q]); }
        float x0, x1;
        reduce12(pr, x0, x1);
        masked_add2(pa0 + (u32)(bs * 16), x0, pa1 + (u32)(bs * 16), x1);
        const bf16x8 z8 = BF8((u32x4{0u, 0u, 0u, 0u}));
        Y0 = MFMA(z8, z8, f32x16{});
        Y1 = MFMA(z8, z8, f32x16{});
      };

      if (ALLOW_FAST && !wide) {
        int base1 = base, base2 = base;
        unsigned long long chg = 0;
        {
          int bb = base;
          for (int i = 1; i < t1 - t0; i++) {
            const int gl1 = __builtin_amdgcn_readlane(glv, i);
            if (gl1 >= bb + 8) { bb = __builtin_amdgcn_readlane(gfv, i); chg |= 1ull << i; }
          }
        }
        load_ap(base1);
        u32 bh = (u32)(base1 + 4 * hh);
        f32x16 XA0, XA1, XB0, XB1;
        const u32 a_ps = pb + ln * 16;
        const u32 a_d2w = d2b + ln * 8;
        const u32 st_row = lds0 + (u32)(L_ST + wv * 2048 + r * 64);
        const u32 a_st = lds0 + (u32)(L_ST + wv * 2048 + (4 * hh + tq) * 64 + (4 * blk + tp) * 8);
        u32 a_pd = lds0 + L_PD, a_lut = lds0 + L_LUT;
        PIN(a_pd); PIN(a_lut);
        u32 a_sp = lds0 + (u32)(L_SG + (t0 * 32 + ln) * 4);
        u32 a_x = hh ? d2b + r * 8 : lds0 + (u32)(L_EA + (t0 * 32 + r) * 8);
        const u32 incB = hh ? (u32)-256 : 256u;
        u32 st_prev = st_row, st_g;
        u32x2 vv;
        u32 sgp;
        auto geom = [&](u32 sg, const f32x4& ps, const f32x4& pd) AI {
          const float dx = pd[0] - ps[0], dy = pd[1] - ps[1], dz = pd[2] - ps[2];
          const bool pad = sg >= (127u << 16);
          u32 g0 = pk_bf16(1.0f, dx), g1 = pk_bf16(dy, dz); PIN(g0); PIN(g1);
          vv[0] = pad ? 0u : g0; vv[1] = pad ? 0u : g1;
          u32x2 d; d[0] = pk_bf16(dx * dx + dy * dy + dz * dz, 0.f); d[1] = d[0];
          stl<u32x2>(a_d2w, d);
          st_g = st_row + (sg >> 16) * 8;
        };
        auto st_write = [&](auto half_c, int bs) AI {
          constexpr int HALF = decltype(half_c)::value;
          u32 a = min(st_g - (u32)(bs * 8), st_row + 56u);
          half_write_b64<HALF>(st_prev, a, vv);
        };
        u32 sg_cur;
        {
          sgp = ldl<u32>(a_sp); a_sp += 256;
          glds_idx(pb, sgp, 16u, 0u, P4);
          const u32 sg0 = ldl<u32>(a_sr); a_sr += 128;
          const f32x4 pd = ldl<f32x4>(a_pd + __builtin_amdgcn_ubfe(sgp, 16, 7) * 16);
          const u32 sgn = ldl<u32>(a_sp); a_sp += 256;
          u32x4 sk; sk_read(a_sk, sk); a_sk += 128;
          sg_cur = ldl<u32>(a_sr); a_sr += 128;
          asm volatile("s_waitcnt vmcnt(0)" ::: "memory");
          bf16x8 A[4];
#pragma unroll
          for (int s = 0; s < 4; s++) A[s] = ldl<bf16x8>(aoff[s]);
          const f32x4 ps = ldl<f32x4>(a_ps);
          asm volatile("s_waitcnt lgkmcnt(0)" ::: "memory");
          issue_h(sk);
          geom(sgp, ps, pd);
          sgp = sgn;
          glds_idx(pb, sgp, 16u, 0u, P4);
          st_write(IC<0>{}, base1);
          const u32x2 xx = ldl<u32x2>(a_x); a_x += 256;
          const u32 t4 = min((sg0 >> 16) - bh, 4u);
          const u32x2 oh = ldl<u32x2>(a_lut + t4 * 8);
          XA0 = f32x16{}; XA1 = f32x16{};
#pragma unroll
          for (int s = 0; s < 4; s++) { XA0 = MFMA(A[s], Bw[s][0], XA0); XA1 = MFMA(A[s], Bw[s][1], XA1); }
          u32x4 a4, b0, b1;
          a4[0] = oh[0]; a4[1] = oh[1]; a4[2] = xx[0]; a4[3] = xx[1];
          b0[0] = ap[0][0]; b0[1] = ap[0][1]; b0[2] = xw[0][0]; b0[3] = xw[0][1];
          b1[0] = ap[1][0]; b1[1] = ap[1][1]; b1[2] = xw[1][0]; b1[3] = xw[1][1];
          XA0 = MFMA(BF8(a4), BF8(b0), XA0); XA1 = MFMA(BF8(a4), BF8(b1), XA1);
          asm volatile("s_nop 7\n\ts_nop 7" : "+v"(XA0), "+v"(XA1));
        }
        auto step = [&](auto next_tag, auto par_c, f32x16& C0, f32x16& C1, f32x16& N0, f32x16& N1, int t) AI {
          constexpr bool NEXT = decltype(next_tag)::value;
          constexpr int PAR = decltype(par_c)::value;
          if (NEXT) {
            if (__builtin_expect((chg >> (t + 1 - t0)) & 1ull, 0)) {
              base1 = __builtin_amdgcn_readlane(gfv, t + 1 - t0); load_ap(base1); bh = (u32)(base1 + 4 * hh);
            }
          }
          u32x4 M[2][2];
          bf16x8 A[4]; u32x4 sk; u32 sr, sgn; f32x4 ps, pd;
          u32x2 xx, oh;
          s16x4 sf[4];
          f32x2 tt[16];
          SB();
#define XP(p) (((p) >> 2) & 1 ? __builtin_shufflevector(C1, C1, 8 * ((p) >> 3) + 2 * ((p) & 3), 8 * ((p) >> 3) + 2 * ((p) & 3) + 1) \
                              : __builtin_shufflevector(C0, C0, 8 * ((p) >> 3) + 2 * ((p) & 3), 8 * ((p) >> 3) + 2 * ((p) & 3) + 1))
#define MSET(p, v) M[((p) >> 2) & 1][((p) >> 3) & 1][(p) & 3] = (v)
          static_for<0, 20>([&](auto ic) AI {
            constexpr int k = decltype(ic)::value - 1;
            constexpr bool R = (k - 1 >= 0 && k - 1 < 16), Cv = (k - 3 >= 0 && k - 3 < 16), E = (k + 1 < 16), Ad = (k >= 0 && k < 16), Mu = (k - 2 >= 0 && k - 2 < 16);
            if constexpr (R && Cv && E && Ad && Mu) {
              const f32x2 xe = XP(k + 1), xm = XP(k - 2);
              u32 mo;
              asm volatile("v_rcp_f32 %0, %0\n\tv_rcp_f32 %1, %1\n\tv_cvt_pk_bf16_f32 %2, %7, %8\n\tv_exp_f32 %3, %9\n\tv_exp_f32 %4, %10\n\t"
                           "v_pk_add_f32 %5, %5, 1.0 op_sel_hi:[1,0]\n\tv_pk_mul_f32 %6, %11, %6"
                           : "+v"(tt[k - 1][0]), "+v"(tt[k - 1][1]), "=&v"(mo), "=&v"(tt[k + 1][0]), "=&v"(tt[k + 1][1]), "+v"(tt[k]), "+v"(tt[k - 2])
                           : "v"(tt[k - 3][0]), "v"(tt[k - 3][1]), "v"(xe[0]), "v"(xe[1]), "v"(xm));
              MSET(k - 3, mo);
            } else {
              if constexpr (R) asm volatile("v_rcp_f32 %0, %0\n\tv_rcp_f32 %1, %1" : "+v"(tt[(k - 1) & 15][0]), "+v"(tt[(k - 1) & 15][1]));
              if constexpr (Cv) {
                u32 mo;
                if constexpr (k == 18) asm volatile("v_cvt_pk_bf16_f32 %0, %1, %2\n\ts_nop 1" : "=v"(mo) : "v"(tt[(k - 3) & 15][0]), "v"(tt[(k - 3) & 15][1]));
                else asm volatile("v_cvt_pk_bf16_f32 %0, %1, %2" : "=v"(mo) : "v"(tt[(k - 3) & 15][0]), "v"(tt[(k - 3) & 15][1]));
                MSET((k - 3) & 15, mo);
              }
              if constexpr (E) {
                const f32x2 xe = XP((k + 1) & 15);
                asm volatile("v_exp_f32 %0, %2\n\tv_exp_f32 %1, %3" : "=&v"(tt[(k + 1) & 15][0]), "=&v"(tt[(k + 1) & 15][1]) : "v"(xe[0]), "v"(xe[1]));
              }
              if constexpr (Ad) asm volatile("v_pk_add_f32 %0, %0, 1.0 op_sel_hi:[1,0]" : "+v"(tt[k & 15]));
              if constexpr (Mu) {
                const f32x2 xm = XP((k - 2) & 15);
                asm volatile("v_pk_mul_f32 %0, %1, %0" : "+v"(tt[(k - 2) & 15]) : "v"(xm));
              }
            }
            SB();
            if constexpr (k == -1) {
#pragma unroll
              for (int u = 0; u < 4; u++) sf[u] = trl(a_st + 512 * u);
              if constexpr (NEXT) {
                if constexpr (PAR == 1) asm volatile("s_waitcnt vmcnt(1)" ::: "memory"); else asm volatile("s_waitcnt vmcnt(0)" ::: "memory");
#pragma unroll
                for (int s = 0; s < 4; s++) A[s] = ldl<bf16x8>(aoff[s]);
                xx = ldl<u32x2>(a_x);
                oh = ldl<u32x2>(a_lut + min((sg_cur >> 16) - bh, 4u) * 8);
                sk_read(a_sk, sk);
                sr = ldl<u32>(a_sr);
              }
            }
            if constexpr (k == 1 && NEXT) {
              asm volatile("s_waitcnt lgkmcnt(0)" ::: "memory");
              issue_h(sk);
              stl<u32x2>(st_prev, u32x2{0u, 0u});
              st_write(IC<1 - PAR>{}, base1);
            }
            if constexpr (NEXT && k >= 2 && k < 12) {
              constexpr int m = k - 2, ks = m >> 1;
              if constexpr ((m & 1) == 0) {
                if constexpr (ks == 0) N0 = MFMA(A[0], Bw[0][0], f32x16{});
                else if constexpr (ks < 4) N0 = MFMA(A[ks & 3], Bw[ks & 3][0], N0);
                else { u32x4 a4, b0; a4[0] = oh[0]; a4[1] = oh[1]; a4[2] = xx[0]; a4[3] = xx[1];
                       b0[0] = ap[0][0]; b0[1] = ap[0][1]; b0[2] = xw[0][0]; b0[3] = xw[0][1]; N0 = MFMA(BF8(a4), BF8(b0), N0); }
              } else {
                if constexpr (ks == 0) N1 = MFMA(A[0], Bw[0][1], f32x16{});
                else if constexpr (ks < 4) N1 = MFMA(A[ks & 3], Bw[ks & 3][1], N1);
                else { u32x4 a4, b1; a4[0] = oh[0]; a4[1] = oh[1]; a4[2] = xx[0]; a4[3] = xx[1];
                       b1[0] = ap[1][0]; b1[1] = ap[1][1]; b1[2] = xw[1][0]; b1[3] = xw[1][1]; N1 = MFMA(BF8(a4), BF8(b1), N1); }
              }
            }
            if constexpr (k == 12) {
              Y0 = MFMA(mk8(sf[0], sf[1]), BF8(M[0][0]), Y0);
              if constexpr (NEXT && PAR == 0) {
                ps = ldl<f32x4>(a_ps);
                pd = ldl<f32x4>(a_pd + __builtin_amdgcn_ubfe(sgp, 16, 7) * 16);
                sgn = ldl<u32>(a_sp);
              }
            }
            if constexpr (k == 13) Y1 = MFMA(mk8(sf[0], sf[1]), BF8(M[1][0]), Y1);
            if constexpr (k == 14 && NEXT && PAR == 0) {
              geom(sgp, ps, pd);
              sgp = sgn; a_sp += 256; PIN(a_sp);
              asm volatile("s_waitcnt lgkmcnt(0)" ::: "memory");
              glds_idx(pb, sgp, 16u, 0u, P4);
            }
            if constexpr (k == 15 && NEXT) {
              sg_cur = sr; a_sk += 128; a_sr += 128; a_x += (PAR == 0 ? incB : 256u); PIN(a_sk); PIN(a_sr); PIN(a_x);
            }
            if constexpr (k == 16) Y0 = MFMA(mk8(sf[2], sf[3]), BF8(M[0][1]), Y0);
            SB();
          });
          Y1 = MFMA(mk8(sf[2], sf[3]), BF8(M[1][1]), Y1);
          SB();
          if (NEXT) { if (__builtin_expect(base1 != base2, 0)) { flush(base2); base2 = base1; } }
        };
        using Tt = std::integral_constant<bool, true>;
        using Ff = std::integral_constant<bool, false>;
        int t = t0;
        while (t + 2 < t1) { step(Tt{}, IC<0>{}, XA0, XA1, XB0, XB1, t); step(Tt{}, IC<1>{}, XB0, XB1, XA0, XA1, t + 1); t += 2; }
        if (t + 1 < t1) { step(Tt{}, IC<0>{}, XA0, XA1, XB0, XB1, t); step(Ff{}, IC<1>{}, XB0, XB1, XA0, XA1, t + 1); }
        else step(Ff{}, IC<0>{}, XA0, XA1, XB0, XB1, t);
        flush(base2);
        asm volatile("s_waitcnt vmcnt(0)" ::: "memory");
      } else {
        u32 slo, slen;
        auto load_sel = [&](int bs) AI {
          const int nl = bs + 4 * blk + tp;
          slo = off[nl]; slen = off[nl + 1] - slo;
        };
        const u32 a_ps = pb + r * 16;
        load_ap(base); load_sel(base);
        u32 sg_cur = ldl<u32>(a_sr); a_sr += 128;
        glds_idx_lo32(pb, sg_cur, P4);
        for (int t = t0; t < t1; t++) {
          const int Tb = t * 32;
          const u32x2 ea = *(const u32x2*)(lds + L_EA + (Tb + r) * 8);
          const f32x4 pd = *(const f32x4*)(lds + L_PD + ((sg_cur >> 16) & 127u) * 16);
          u32x4 sk; sk_read(a_sk, sk);
          const u32 sr = ldl<u32>(a_sr);
          asm volatile("s_waitcnt vmcnt(0)" ::: "memory");
          bf16x8 A[4];
#pragma unroll
          for (int s = 0; s < 4; s++) A[s] = ldl<bf16x8>(aoff[s]);
          const f32x4 ps = ldl<f32x4>(a_ps);
          asm volatile("s_waitcnt lgkmcnt(0)" ::: "memory");
          issue_h(sk);
          glds_idx_lo32(pb, sr, P4);
          const u32 sg = sg_cur;
          const float dx = pd[0] - ps[0], dy = pd[1] - ps[1], dz = pd[2] - ps[2];
          { u32x2 v; v[0] = pk_bf16(1.0f, dx); v[1] = pk_bf16(dy, dz); stl<u32x2>(a_vw, v); }
          const u32 d2 = pk_bf16(dx * dx + dy * dy + dz * dz, 0.f);
          sg_cur = sr; a_sk += 128; a_sr += 128;
          const int g = min((int)(sg >> 16), glast);
          const int gf = __builtin_amdgcn_readlane(g, 0), gl = __builtin_amdgcn_readlane(g, 31);
          if (gl >= base + 8) { flush(base); base = gf; load_ap(base); load_sel(base); }
          f32x16 X0 = {}, X1 = {};
#pragma unroll
          for (int s = 0; s < 4; s++) { X0 = MFMA(A[s], Bw[s][0], X0); X1 = MFMA(A[s], Bw[s][1], X1); }
          for (int wb = base;; wb += 8) {
            if (wb != base) load_ap(wb);
            const u32 t4 = (u32)g - (u32)(wb + 4 * hh);
            unsigned long long oh = 0x3F80ull << ((t4 & 3u) * 16u);
            if (t4 >= 4u) oh = 0ull;
            u32x4 a4; a4[0] = (u32)oh; a4[1] = (u32)(oh >> 32);
            a4[2] = wb == base ? (hh ? d2 : ea[0]) : 0u; a4[3] = wb == base ? (hh ? d2 : ea[1]) : 0u;
            u32x4 b0, b1;
            b0[0] = ap[0][0]; b0[1] = ap[0][1]; b0[2] = xw[0][0]; b0[3] = xw[0][1];
            b1[0] = ap[1][0]; b1[1] = ap[1][1]; b1[2] = xw[1][0]; b1[3] = xw[1][1];
            X0 = MFMA(BF8(a4), BF8(b0), X0);
            X1 = MFMA(BF8(a4), BF8(b1), X1);
            if (gl < wb + 8) { if (wb != base) load_ap(base); break; }
          }
          u32x4 M[2][2];
#pragma unroll
          for (int s = 0; s < 2; s++)
#pragma unroll
            for (int j = 0; j < 4; j++) {
              M[0][s][j] = silu2(X0[8 * s + 2 * j], X0[8 * s + 2 * j + 1]);
              M[1][s][j] = silu2(X1[8 * s + 2 * j], X1[8 * s + 2 * j + 1]);
            }
          for (;;) {
            s16x4 sf[4];
            const u32 d0 = (u32)(Tb + 4 * hh + tq) - slo;
#pragma unroll
            for (int u = 0; u < 4; u++) sf[u] = trl(((d0 + 8u * u) < slen ? a_vr : a_z) + 64 * u);
#pragma unroll
            for (int s = 0; s < 2; s++) {
              const bf16x8 S = mk8(sf[2 * s], sf[2 * s + 1]);
              Y0 = MFMA(S, BF8(M[0][s]), Y0);
              Y1 = MFMA(S, BF8(M[1][s]), Y1);
            }
            if (gl < base + 8) break;
            flush(base); base += 8; load_ap(base); load_sel(base);
          }
        }
        flush(base);
        asm volatile("s_waitcnt vmcnt(0)" ::: "memory");
      }
    }
    if (ALLOW_FAST) {
      glds16(lds0 + L_GB + wv * 4096, (u32)(wv * 2048 + lane * 16), wh);
      glds16(lds0 + L_GB + wv * 4096 + 1024, (u32)(wv * 2048 + 1024 + lane * 16), wh);
    }
    if (lane == 0) misc[16 + slot] = (u32)head;
    BAR();
    {
      float* hb = (float*)(lds + L_HB) + wv * MS;
      const int hprev = slot > 0 ? (int)misc[16 + slot - 1] : 127, hnext = slot < 7 ? (int)misc[16 + slot + 1] : 127;
      const bool uniq = (hprev != head) && (hnext != head);
      if (head != 127) {
        const float v = hb[lane];
        hb[lane] = 0.f;
        if (uniq) macc[head * MS + lane] += v;
        else atomicAdd(&macc[head * MS + lane], v);
      }
      if (tid < 144) hist[tid] = 0;
    }
  };

  load_records(0, 4 * MAXR);
  int Pmax = 0, ntot = 0;
  {
    for (int i = tid; i < (99 + 8) * MS / 4; i += 512) *(f32x4*)(macc + 4 * i) = f32x4{0.f, 0.f, 0.f, 0.f};
    if (tid < 105) *(f32x4*)(pacc + 4 * tid) = f32x4{0.f, 0.f, 0.f, 0.f};
    if (tid < 64) *(u32*)(lds + L_ZERO + tid * 4) = 0;
    if (tid < 5) { u32x2 v; v[0] = tid == 0 ? 0x3F80u : (tid == 1 ? 0x3F800000u : 0u); v[1] = tid == 2 ? 0x3F80u : (tid == 3 ? 0x3F800000u : 0u); *(u32x2*)(lds + L_LUT + tid * 8) = v; }
#pragma unroll
    for (int i = 0; i < 2; i++) *(u32x4*)(lds + L_ST + (tid + 512 * i) * 16) = u32x4{0, 0, 0, 0};
    if (tid < 128) { f32x4 v; v[0] = bf2f(pd0); v[1] = bf2f(pd1); v[2] = bf2f(pd2); v[3] = 0.f; *(f32x4*)(lds + L_PD + tid * 16) = v; }
    if (tid < 144) hist[tid] = 0;
    if (lane == 0) { misc[wv] = (u32)P; misc[8 + wv] = (u32)csum; }
    u32x4 o;
#pragma unroll
    for (int i = 0; i < 4; i++) o[i] = pk_bf16(bflo(wB[i]) * NLOG2E, bfhi(wB[i]) * NLOG2E);
    const u32x4 z4 = {0, 0, 0, 0};
    *(u32x4*)(lds + L_WE + tid * 16) = wA;
    *(u32x4*)(lds + L_WE + 8192 + tid * 16) = o;
    if (tid < 40) *(u32x4*)(lds + L_WE + 16384 + tid * 16) = wC;
    *(u32x4*)(lds + L_H2 + tid * 16) = tid < nn * 8 ? hA : z4;
    if (tid < 288) *(u32x4*)(lds + L_H2 + 8192 + tid * 16) = tid + 512 < nn * 8 ? hB : z4;
    BAR();
#pragma unroll
    for (int i = 0; i < 8; i++) { Pmax = max(Pmax, (int)misc[i]); ntot += (int)misc[8 + i]; }
    Pmax = __builtin_amdgcn_readfirstlane(Pmax); ntot = __builtin_amdgcn_readfirstlane(ntot);
#pragma unroll
    for (int s = 0; s < 4; s++)
#pragma unroll
      for (int ct = 0; ct < 2; ct++) {
        const u32 p = lds0 + (u32)(L_WE + (64 + 32 * hh + 8 * s + tq) * 128 + (32 * ct + 16 * blk + 4 * tp) * 2);
        Bw[s][ct] = mk8(trl(p), trl(p + 4 * 128));
      }
#pragma unroll
    for (int ct = 0; ct < 2; ct++) {
      const u16* wt = (const u16*)(lds + L_WE + 16384);
      const int col = 32 * ct + r;
      const float w0 = bf2f(wt[col]) * NLOG2E, w1 = bf2f(wt[64 + col]) * NLOG2E, w2 = bf2f(wt[128 + col]) * NLOG2E,
                  w3 = bf2f(wt[192 + col]) * NLOG2E, w4 = bf2f(wt[256 + col]) * NLOG2E;
      const u32 hi = pk_bf16(w0, 0.f) & 0xffffu;
      const u32 lo = pk_bf16(w0 - bf2f(hi), 0.f) & 0xffffu;
      xw[ct][0] = hh ? hi : pk_bf16(w1, w2);
      xw[ct][1] = hh ? lo : pk_bf16(w3, w4);
    }
    {
      const int nt = wv >> 1, ct = wv & 1;
      f32x16 acc = {};
#pragma unroll
      for (int s = 0; s < 4; s++) {
        const u32 p = lds0 + (u32)(L_WE + (32 * hh + 8 * s + tq) * 128 + (32 * ct + 16 * blk + 4 * tp) * 2);
        bf16x8 aw = mk8(trl(p), trl(p + 4 * 128));
        bf16x8 bh = *(const bf16x8*)(lds + L_H2 + min(32 * nt + r, 99) * 128 + (4 * hh + s) * 16);
        acc = MFMA(aw, bh, acc);
      }
      const int node = 32 * nt + r;
      char* ap = lds + (node < 105 ? L_AP + node * 128 : L_DESC + 2048 + wv * 128) + (32 * ct + 4 * hh) * 2;
#pragma unroll
      for (int q = 0; q < 4; q++) {
        u32x2 v;
        v[0] = pk_bf16(acc[4 * q] * NLOG2E, acc[4 * q + 1] * NLOG2E);
        v[1] = pk_bf16(acc[4 * q + 2] * NLOG2E, acc[4 * q + 3] * NLOG2E);
        *(u32x2*)(ap + 16 * q) = v;
      }
    }
  }
  if (Pmax <= 4 * MAXR && ntot <= CAP) {
    process(std::integral_constant<bool, true>{});
  } else {
    for (int plo = 0; plo < Pmax; plo += FPPB) {
      BAR();
      load_records(plo, FPPB);
      process(std::integral_constant<bool, false>{});
    }
  }

  if (!(Pmax <= 4 * MAXR && ntot <= CAP)) {
    glds16(lds0 + L_GB + wv * 4096, (u32)(wv * 2048 + lane * 16), wh);
    glds16(lds0 + L_GB + wv * 4096 + 1024, (u32)(wv * 2048 + 1024 + lane * 16), wh);
  }
  asm volatile("s_waitcnt vmcnt(0)" ::: "memory");
  BAR();
  {
    int ln = lane;
    asm volatile("" : "+v"(ln));
    const int r = ln & 31, hh = ln >> 5;
    const int G4 = ln >> 4, i16 = ln & 15, tq = i16 >> 2, tp = i16 & 3, blk = G4 & 1;
    const int nt = wv >> 1, ct = wv & 1;
    const int node = 32 * nt + r;
    const int nrow = node < NB ? node : MDUMMY;
    f32x16 acc = {};
#pragma unroll
    for (int s = 0; s < 8; s++) {
      const int kb = (s < 4) ? (32 * hh + 8 * s) : (64 + 32 * hh + 8 * (s - 4));
      const int k0 = kb + tq;
      const u32 p = lds0 + (u32)(L_GB + (k0 >> 4) * 4096 + (k0 & 15) * 128 + (32 * ct + 16 * blk + 4 * tp) * 2);
      const bf16x8 aw = mk8(trl(p), trl(p + 4 * 128));
      bf16x8 bb;
      if (s < 4) {
        bb = *(const bf16x8*)(lds + L_H2 + min(node, 99) * 128 + (4 * hh + s) * 16);
      } else {
        const float* mp = macc + nrow * MS + 32 * hh + 8 * (s - 4);
        const f32x4 m0 = *(const f32x4*)mp, m1 = *(const f32x4*)(mp + 4);
        u32x4 pk;
        pk[0] = pk_bf16(m0[0] * NLN2, m0[1] * NLN2); pk[1] = pk_bf16(m0[2] * NLN2, m0[3] * NLN2);
        pk[2] = pk_bf16(m1[0] * NLN2, m1[1] * NLN2); pk[3] = pk_bf16(m1[2] * NLN2, m1[3] * NLN2);
        bb = __builtin_bit_cast(bf16x8, pk);
      }
      acc = MFMA(aw, bb, acc);
    }
#pragma unroll
    for (int q = 0; q < 4; q++) {
      float u[4];
#pragma unroll
      for (int i = 0; i < 4; i++) {
        const float x = bf2f(pk_bf16(acc[4 * q + i], 0.f) & 0xffffu);
        u[i] = x * __builtin_amdgcn_rcpf(1.0f + __builtin_amdgcn_exp2f(x * NLOG2E));
      }
      u32x2 v; v[0] = pk_bf16(u[0], u[1]); v[1] = pk_bf16(u[2], u[3]);
      *(u32x2*)(lds + L_UT + node * 144 + (32 * ct + 8 * q + 4 * hh) * 2) = v;
    }
  }
  BAR();
#pragma unroll
  for (int k = 0; k < 2; k++) {
    const int i = tid + 512 * k;
    if (i < nn * 8) {
      const int row = i >> 3, chn = i & 7;
      const u32x4 u = *(const u32x4*)(lds + L_UT + row * 144 + chn * 16);
      const u32x4 hv = *(const u32x4*)(lds + L_H2 + i * 16);
      u32x4 o;
#pragma unroll
      for (int j = 0; j < 4; j++) o[j] = pk_bf16(bflo(hv[j]) + bflo(u[j]), bfhi(hv[j]) + bfhi(u[j]));
      *(u32x4*)(out + (size_t)(node0 + row) * 64 + chn * 8) = o;
    }
  }
  if (tid < nn * 3) {
    const int nd = tid / 3, c = tid - nd * 3;
    const float pv = *(const float*)(lds + L_PD + nd * 16 + c * 4);
    const float dlt = bf2f(pk_bf16(pacc[nd * 4 + c], 0.f) & 0xffffu);
    out[(size_t)N * 64 + (size_t)node0 * 3 + tid] = (u16)(pk_bf16(pv + dlt, 0.f) & 0xffffu);
  }
}

extern "C" void kernel_launch(void* const* d_in, const int* in_sizes, int n_in, void* d_out, int out_size, void* d_ws,
                              size_t ws_size, hipStream_t stream) {
  const u16* h = (const u16*)d_in[0];
  const u16* pos = (const u16*)d_in[1];
  const u16* ea = (const u16*)d_in[2];
  const int* src = (const int*)d_in[3];
  const int* dst = (const int*)d_in[4];
  const u16* we = (const u16*)d_in[5];
  const u16* wx = (const u16*)d_in[6];
  const u16* wh = (const u16*)d_in[7];
  const int N = in_sizes[0] / 64, E = in_sizes[3];
  char* ws = (char*)d_ws;
  u32* R = (u32*)ws;
  f32x4* P4 = (f32x4*)(ws + (size_t)10 * 1048576);
  u32* T = (u32*)(ws + (size_t)11 * 1048576);
  (void)hipFuncSetAttribute((const void*)k1_partition, hipFuncAttributeMaxDynamicSharedMemorySize, K1_LDS);
  (void)hipFuncSetAttribute((const void*)k2_main, hipFuncAttributeMaxDynamicSharedMemorySize, K2_LDS);
  k1_partition<<<(E + CH - 1) / CH, 1024, K1_LDS, stream>>>(pos, (const u32x4*)ea, (const i32x4*)src, (const i32x4*)dst, R, P4, T, N, E);
  k2_main<<<(N + NB - 1) / NB, 512, K2_LDS, stream>>>(h, pos, we, wx, wh, R, P4, T, (u16*)d_out, N);
}
```

```cpp
#include <hip/hip_runtime.h>
#include <stdint.h>
#include <type_traits>

typedef unsigned short u16;
typedef unsigned char u8;
typedef unsigned int u32;
typedef short s16x4 __attribute__((ext_vector_type(4)));
typedef __bf16 bf16x2 __attribute__((ext_vector_type(2)));
typedef __bf16 bf16x8 __attribute__((ext_vector_type(8)));
typedef float f32x2 __attribute__((ext_vector_type(2)));
typedef float f32x4 __attribute__((ext_vector_type(4)));
typedef float f32x16 __attribute__((ext_vector_type(16)));
typedef u32 u32x2 __attribute__((ext_vector_type(2)));
typedef u32 u32x3 __attribute__((ext_vector_type(3)));
typedef u32 u32x4 __attribute__((ext_vector_type(4)));
typedef int i32x4 __attribute__((ext_vector_type(4)));
#define LAS __attribute__((address_space(3)))
#define DI __device__ __forceinline__
#define AI __attribute__((always_inline))

constexpr int NB    = 98;
constexpr int CH    = 3128;
constexpr int NCH   = 256;
constexpr int CAP   = 3392;
constexpr int MAXR  = 12;
constexpr int FPPB  = 26;
#define SPLIT 86
constexpr float NLOG2E = -1.4426950408889634f;
constexpr float NLN2   = -0.6931471805599453f;

DI float bf2f(u32 bits16) { return __uint_as_float(bits16 << 16); }
DI float bflo(u32 v) { return __uint_as_float(v << 16); }
DI float bfhi(u32 v) { return __uint_as_float(v & 0xffff0000u); }
DI u32 pk_bf16(float lo, float hi) {
  f32x2 v; v[0] = lo; v[1] = hi;
  return __builtin_bit_cast(u32, __builtin_convertvector(v, bf16x2));
}
template <int CTRL, int RMASK> DI int dpp_i(int x) { return __builtin_amdgcn_update_dpp(0, x, CTRL, RMASK, 0xf, false); }
template <int CTRL> DI float dpp_f(float x) {
  return __builtin_bit_cast(float, __builtin_amdgcn_update_dpp(0, __builtin_bit_cast(int, x), CTRL, 0xf, 0xf, true));
}
DI int wave_incl_scan(int x) {
  x += dpp_i<0x111, 0xf>(x); x += dpp_i<0x112, 0xf>(x); x += dpp_i<0x114, 0xf>(x); x += dpp_i<0x118, 0xf>(x);
  x += dpp_i<0x142, 0xa>(x); x += dpp_i<0x143, 0xc>(x);
  return x;
}
DI float half_reduce(float x) {
  x += dpp_f<0x111>(x); x += dpp_f<0x112>(x); x += dpp_f<0x114>(x); x += dpp_f<0x118>(x);
  x += dpp_f<0x142>(x);
  return x;
}
DI void glds16(u32 lds_addr, u32 voff, const void* base) {
  asm volatile("s_mov_b32 m0, %0\n\ts_nop 0\n\tglobal_load_lds_dwordx4 %1, %2" ::"s"(lds_addr), "v"(voff), "s"(base) : "memory");
}
DI void glds16_lo32(u32 lds_addr, u32 voff, const void* base) {
  asm volatile("s_mov_b32 m0, %0\n\ts_mov_b64 exec, 0xffffffff\n\tglobal_load_lds_dwordx4 %1, %2\n\ts_mov_b64 exec, -1"
               ::"s"(lds_addr), "v"(voff), "s"(base) : "memory");
}
DI void glds_idx(u32 lds_addr, u32 idx, u32 mul, u32 add, const void* base) {
  u32 t;
  asm volatile("s_mov_b32 m0, %1\n\tv_mad_u32_u16 %0, %2, %3, %4\n\tglobal_load_lds_dwordx4 %0, %5"
               : "=&v"(t) : "s"(lds_addr), "v"(idx), "s"(mul), "v"(add), "s"(base) : "memory");
}
DI void glds_idx_lo32(u32 lds_addr, u32 idx, const void* base) {
  u32 t;
  asm volatile("s_mov_b32 m0, %1\n\tv_mad_u32_u16 %0, %2, 16, 0\n\ts_mov_b64 exec, 0xffffffff\n\tglobal_load_lds_dwordx4 %0, %3\n\ts_mov_b64 exec, -1"
               : "=&v"(t) : "s"(lds_addr), "v"(idx), "s"(base) : "memory");
}
DI u32 mad16(u32 a_lo16, u32 b, u32 c) {
  u32 d;
  asm("v_mad_u32_u16 %0, %1, %2, %3" : "=v"(d) : "v"(a_lo16), "s"(b), "v"(c));
  return d;
}
template <int HALF> DI void half_write_b64(u32& prev, u32 a, u32x2 v) {
  if constexpr (HALF == 0)
    asm volatile("s_mov_b64 exec, 0xffffffff\n\tds_write_b64 %1, %2\n\tv_mov_b32 %0, %1\n\ts_mov_b64 exec, -1" : "+v"(prev) : "v"(a), "v"(v) : "memory");
  else
    asm volatile("s_mov_b64 exec, %3\n\tds_write_b64 %1, %2\n\tv_mov_b32 %0, %1\n\ts_mov_b64 exec, -1" : "+v"(prev) : "v"(a), "v"(v), "s"(0xffffffff00000000ull) : "memory");
}
DI void reduce12(float (&v)[12], float& x0, float& x1) {
  asm volatile(
      "s_nop 1\n\t"
      "v_permlane16_swap_b32 %0, %1\n\tv_permlane16_swap_b32 %2, %3\n\tv_permlane16_swap_b32 %4, %5\n\t"
      "v_permlane16_swap_b32 %6, %7\n\tv_permlane16_swap_b32 %8, %9\n\tv_permlane16_swap_b32 %10, %11\n\t"
      "s_nop 1\n\t"
      "v_add_f32 %0, %0, %1\n\tv_add_f32 %2, %2, %3\n\tv_add_f32 %4, %4, %5\n\t"
      "v_add_f32 %6, %6, %7\n\tv_add_f32 %8, %8, %9\n\tv_add_f32 %10, %10, %11\n\t"
      "v_add_f32_dpp %1, %0, %0 row_ror:8 row_mask:0xf bank_mask:0x3\n\t"
      "v_add_f32_dpp %3, %4, %4 row_ror:8 row_mask:0xf bank_mask:0x3\n\t"
      "v_add_f32_dpp %5, %8, %8 row_ror:8 row_mask:0xf bank_mask:0x3\n\t"
      "v_add_f32_dpp %1, %2, %2 row_ror:8 row_mask:0xf bank_mask:0xc\n\t"
      "v_add_f32_dpp %3, %6, %6 row_ror:8 row_mask:0xf bank_mask:0xc\n\t"
      "v_add_f32_dpp %5, %10, %10 row_ror:8 row_mask:0xf bank_mask:0xc\n\t"
      "v_add_f32_dpp %7, %1, %1 row_half_mirror row_mask:0xf bank_mask:0x5\n\t"
      "v_add_f32_dpp %7, %3, %3 row_half_mirror row_mask:0xf bank_mask:0xa\n\t"
      "v_add_f32_dpp %9, %5, %5 row_half_mirror row_mask:0xf bank_mask:0xf\n\t"
      "s_nop 1\n\t"
      "v_add_f32_dpp %7, %7, %7 quad_perm:[1,0,3,2] row_mask:0xf bank_mask:0xf\n\t"
      "v_add_f32_dpp %9, %9, %9 quad_perm:[1,0,3,2] row_mask:0xf bank_mask:0xf\n\t"
      "s_nop 1\n\t"
      "v_add_f32_dpp %7, %7, %7 quad_perm:[2,3,0,1] row_mask:0xf bank_mask:0xf\n\t"
      "v_add_f32_dpp %9, %9, %9 quad_perm:[2,3,0,1] row_mask:0xf bank_mask:0xf\n\t"
      "s_nop 0"
      : "+v"(v[0]), "+v"(v[1]), "+v"(v[2]), "+v"(v[3]), "+v"(v[4]), "+v"(v[5]), "+v"(v[6]), "+v"(v[7]), "+v"(v[8]), "+v"(v[9]),
        "+v"(v[10]), "+v"(v[11]));
  x0 = v[7]; x1 = v[9];
}
DI void masked_add2(u32 a0, float x0, u32 a1, float x1) {
  asm volatile("s_mov_b64 exec, %4\n\tds_add_f32 %0, %1\n\ts_mov_b64 exec, %5\n\tds_add_f32 %2, %3\n\ts_mov_b64 exec, -1"
               ::"v"(a0), "v"(x0), "v"(a1), "v"(x1), "s"(0x1111111111111111ull), "s"(0x0101010101010101ull) : "memory");
}
template <class T> DI T ldl(u32 a) { return *(const LAS T*)(uintptr_t)a; }
template <class T> DI void stl(u32 a, T v) { *(LAS T*)(uintptr_t)a = v; }
DI s16x4 trl(u32 a) { return __builtin_amdgcn_ds_read_tr16_b64_v4i16((LAS s16x4*)(uintptr_t)a); }
DI bf16x8 mk8(s16x4 lo, s16x4 hi) {
  return __builtin_bit_cast(bf16x8, __builtin_shufflevector(lo, hi, 0, 1, 2, 3, 4, 5, 6, 7));
}
#define BAR() do { asm volatile("s_waitcnt lgkmcnt(0)" ::: "memory"); __builtin_amdgcn_s_barrier(); asm volatile("" ::: "memory"); } while (0)
#define SB() __builtin_amdgcn_sched_barrier(0)
#define PIN(x) asm volatile("" : "+v"(x))
template <int I, int N, class F> DI void static_for(F&& f) {
  if constexpr (I < N) { f(std::integral_constant<int, I>{}); static_for<I + 1, N>(f); }
}
template <int I> using IC = std::integral_constant<int, I>;
#define MFMA(a, b, c) __builtin_amdgcn_mfma_f32_32x32x16_bf16((a), (b), (c), 0, 0, 0)
#define BF8(x) __builtin_bit_cast(bf16x8, (x))

constexpr int K1_HIST = CH * 12 + 64;
constexpr int K1_OFF = K1_HIST + 1024 + 64;
constexpr int K1_LDS = K1_OFF + 1024 + 64;

__global__ __launch_bounds__(1024) void k1_partition(const u16* __restrict__ pos, const u32x4* __restrict__ ea4,
                                                     const i32x4* __restrict__ src4, const i32x4* __restrict__ dst4,
                                                     u32* __restrict__ R, f32x4* __restrict__ P4, u32* __restrict__ T,
                                                     int N, int E) {
  extern __shared__ __attribute__((aligned(16))) char lds[];
  const int tid = threadIdx.x, lane = tid & 63;
  const int wv = __builtin_amdgcn_readfirstlane(tid >> 6);
  const int w = blockIdx.x;
  u32* hist = (u32*)(lds + K1_HIST);
  u32* off = (u32*)(lds + K1_OFF);
  int cnt = E - w * CH; cnt = cnt > CH ? CH : cnt; cnt = cnt < 4 ? 4 : cnt;
  const bool act = 4 * tid < cnt;
  const int q = (w * CH >> 2) + (act ? tid : 0);
  const i32x4 d4 = __builtin_nontemporal_load(&dst4[q]);
  const i32x4 s4 = __builtin_nontemporal_load(&src4[q]);
  const u32x4 a0 = __builtin_nontemporal_load(&ea4[2 * q]);
  const u32x4 a1 = __builtin_nontemporal_load(&ea4[2 * q + 1]);
  {
    const int n = w * NB + (tid - 896);
    if (tid >= 896 && tid < 896 + NB && n < N) {
      const u16* p = pos + (size_t)n * 3;
      f32x4 v; v[0] = bf2f(p[0]); v[1] = bf2f(p[1]); v[2] = bf2f(p[2]); v[3] = 0.f;
      P4[n] = v;
    }
  }
  if (tid < 272) hist[tid] = 0;
  BAR();
  int bk[4], gl[4], rk[4];
#pragma unroll
  for (int i = 0; i < 4; i++) {
    const u32 d = (u32)d4[i];
    bk[i] = (int)((d * 42800u) >> 22);
    gl[i] = (int)d - bk[i] * NB;
    bk[i] = act ? bk[i] : 256 + (lane & 15);
    rk[i] = atomicAdd(&hist[bk[i]], 1u);
  }
  BAR();
  {
    u32x4 c = *(const u32x4*)(hist + 4 * lane);
    int s = c[0] + c[1] + c[2] + c[3];
    int inc = wave_incl_scan(s);
    u32x4 o;
    o[0] = inc - s; o[1] = o[0] + c[0]; o[2] = o[1] + c[1]; o[3] = o[2] + c[2];
    *(u32x4*)(off + 4 * lane) = o;
    if (wv == 15) {
#pragma unroll
      for (int i = 0; i < 4; i++) T[(4 * lane + i) * NCH + w] = o[i] | (c[i] << 16);
    }
  }
  if (act) {
#pragma unroll
    for (int i = 0; i < 4; i++) {
      const int slot = (int)(off[bk[i]] + rk[i]);
      u32* p = (u32*)(lds + slot * 12);
      p[0] = i < 2 ? a0[2 * i] : a1[2 * i - 4];
      p[1] = i < 2 ? a0[2 * i + 1] : a1[2 * i - 3];
      p[2] = (u32)s4[i] | ((u32)gl[i] << 16);
    }
  }
  BAR();
  u32x4* Rw = (u32x4*)(R + (size_t)w * CH * 3);
  const int n16 = (cnt * 12 + 15) >> 4;
#pragma unroll
  for (int k = 0; k < 3; k++) {
    const int i = tid + 1024 * k;
    if (i < n16) __builtin_nontemporal_store(*(const u32x4*)(lds + i * 16), &Rw[i]);
  }
}

constexpr int L_GB = 0;
constexpr int L_PB = L_GB + 8 * 4096;
constexpr int L_D2 = L_PB + 8 * 1024;
constexpr int L_ST = L_D2 + 8 * 512;
constexpr int L_EA = L_ST + 8 * 2048;
constexpr int L_SG = L_EA + (CAP + 32) * 8;
constexpr int SGPAD = 128;
constexpr int L_AP = L_SG + (CAP + 160) * 4;
constexpr int MS = 68;
constexpr int MDUMMY = 98;
constexpr int L_MACC = L_AP + 105 * 128;
constexpr int L_HB = L_MACC + 99 * MS * 4;
constexpr int L_PACC = L_HB + 8 * MS * 4;
constexpr int L_HIST = L_PACC + 105 * 16;
constexpr int L_OFF = L_HIST + 144 * 4;
constexpr int L_ZERO = L_OFF + 144 * 4;
constexpr int L_LUT = L_ZERO + 256;
constexpr int L_PD = L_LUT + 64;
constexpr int L_MISC = L_PD + 128 * 16;
constexpr int L_H2 = L_MISC + 256;
constexpr int K2_LDS = L_H2 + 12800;
static_assert(K2_LDS <= 163840, "LDS");
static_assert(L_AP % 16 == 0 && L_MACC % 16 == 0 && L_EA % 16 == 0 && L_SG % 16 == 0 && L_HB % 16 == 0 && L_PD % 16 == 0 && L_H2 % 16 == 0, "align");
constexpr int L_WE = L_EA;
constexpr int L_DESC = L_GB;
constexpr int L_UT = L_EA;
constexpr int L_WH = L_EA + 128 * 144;

DI u32 silu2(float z0, float z1) {
  f32x2 z; z[0] = z0; z[1] = z1;
  f32x2 t; t[0] = __builtin_amdgcn_exp2f(z0); t[1] = __builtin_amdgcn_exp2f(z1);
  t = t + 1.0f;
  f32x2 r; r[0] = __builtin_amdgcn_rcpf(t[0]); r[1] = __builtin_amdgcn_rcpf(t[1]);
  f32x2 m = z * r;
  return __builtin_bit_cast(u32, __builtin_convertvector(m, bf16x2));
}

__global__ __launch_bounds__(512) void k2_main(const u16* __restrict__ h, const u16* __restrict__ pos,
                                               const u16* __restrict__ we, const u16* __restrict__ wx,
                                               const u16* __restrict__ wh, const u32* __restrict__ R,
                                               const f32x4* __restrict__ P4, const u32* __restrict__ T,
                                               u16* __restrict__ out, int N) {
  extern __shared__ __attribute__((aligned(16))) char lds[];
  const int tid = threadIdx.x, lane = tid & 63;
  const int wv = __builtin_amdgcn_readfirstlane(tid >> 6);
  const int b = blockIdx.x;
  const int node0 = b * NB;
  int nn = N - node0; nn = nn > NB ? NB : nn; nn = nn < 1 ? 1 : nn;
  const int r = lane & 31, hh = lane >> 5;
  const u32 lds0 = (u32)(uintptr_t)(LAS char*)lds;
  u32* hist = (u32*)(lds + L_HIST);
  u32* off = (u32*)(lds + L_OFF);
  float* macc = (float*)(lds + L_MACC);
  float* pacc = (float*)(lds + L_PACC);
  u32* misc = (u32*)(lds + L_MISC);

  const u32 tv = T[b * NCH + wv * 32 + r];
  const u32x4* we16 = (const u32x4*)we;
  const u32x4 wA = we16[tid], wB = we16[512 + tid];
  const u32x4 wC = we16[1024 + (tid < 40 ? tid : 0)];
  const u32x4* h16 = (const u32x4*)(h + (size_t)node0 * 64);
  const u32x4 hA = h16[tid < nn * 8 ? tid : 0];
  const u32x4 hB = h16[tid + 512 < nn * 8 ? tid + 512 : 0];
  const u32x4* wh16 = (const u32x4*)wh;
  const u16* pp = pos + (size_t)(node0 + (tid < nn ? tid : 0)) * 3;
  const u32 pd0 = pp[0], pd1 = pp[1], pd2 = pp[2];
  const float wx0 = bf2f(wx[r]) * NLN2, wx1 = bf2f(wx[32 + r]) * NLN2;

  const u32 cstart = tv & 0xffffu;
  const u32 ccnt = lane < 32 ? (tv >> 16) : 0u;
  const int np = (int)((ccnt + 15) >> 4);
  const int pinc = wave_incl_scan(np);
  const int pexc = pinc - np;
  const int P = __builtin_amdgcn_readlane(pinc, 31);
  const int csum = __builtin_amdgcn_readlane(wave_incl_scan((int)ccnt), 31);

  const int G4 = lane >> 4, i16 = lane & 15, tq = i16 >> 2, tp = i16 & 3, blk = G4 & 1;
  bf16x8 Bw[4][2];
  u32 xw[2][2];
  u32x3 rec[MAXR];
  u32 vmask = 0;
  int nrounds = 0;
#pragma unroll
  for (int rr = 8; rr < MAXR; rr++) rec[rr] = u32x3{0, 0, 0};
  u32* desc = (u32*)(lds + L_DESC + wv * 256);
  auto load_records = [&](int plo, int ppb) AI {
    int k0 = plo - pexc; k0 = k0 < 0 ? 0 : k0;
    int k1 = plo + ppb - pexc; k1 = k1 > np ? np : k1;
    const u32 gbase = (u32)((wv * 32 + r) * CH) + cstart;
    for (int k = k0; k < k1; k++) {
      u32 nrec = ccnt - 16 * k; nrec = nrec > 16 ? 16 : nrec;
      desc[pexc + k - plo] = (gbase + 16 * k) | (nrec << 24);
    }
    int Pb = P - plo; Pb = Pb < 0 ? 0 : Pb; Pb = Pb > ppb ? ppb : Pb;
    nrounds = (Pb + 3) >> 2;
    vmask = 0;
    auto rounds = [&](auto lo_c, auto hi_c) AI {
      constexpr int lo = decltype(lo_c)::value, hi = decltype(hi_c)::value;
      u32 dd[hi - lo];
#pragma unroll
      for (int rr = lo; rr < hi; rr++) dd[rr - lo] = desc[4 * rr + G4];
#pragma unroll
      for (int rr = lo; rr < hi; rr++) {
        const u32 d = (4 * rr + G4 < Pb) ? dd[rr - lo] : 0u;
        const bool v = (u32)i16 < (d >> 24);
        const u32 gi = (d & 0xffffffu) + (v ? i16 : 0);
        rec[rr] = __builtin_nontemporal_load((const u32x3*)(R + (size_t)gi * 3));
        vmask |= v ? (1u << rr) : 0u;
      }
    };
    rounds(IC<0>{}, IC<8>{});
    if (nrounds > 8) rounds(IC<8>{}, IC<10>{});
    if (nrounds > 10) rounds(IC<10>{}, IC<12>{});
  };
  auto process = [&](auto fast_tag) AI {
    constexpr bool ALLOW_FAST = decltype(fast_tag)::value;
    u32 rk[MAXR];
    auto ranks = [&](auto lo_c, auto hi_c) AI {
      constexpr int lo = decltype(lo_c)::value, hi = decltype(hi_c)::value;
#pragma unroll
      for (int rr = lo; rr < hi; rr++) {
        const u32 g = (vmask >> rr) & 1u ? (rec[rr][2] >> 16) : 128u + (lane & 15);
        rk[rr] = atomicAdd(&hist[g], 1u);
      }
    };
#pragma unroll
    for (int rr = 8; rr < MAXR; rr++) rk[rr] = 0;
    ranks(IC<0>{}, IC<8>{});
    if (nrounds > 8) ranks(IC<8>{}, IC<10>{});
    if (nrounds > 10) ranks(IC<10>{}, IC<12>{});
    BAR();
    int n, glast;
    {
      u32x2 c = *(const u32x2*)(hist + 2 * lane);
      int s = c[0] + c[1];
      int inc = wave_incl_scan(s);
      u32x2 o; o[0] = inc - s; o[1] = o[0] + c[0];
      *(u32x2*)(off + 2 * lane) = o;
      n = __builtin_amdgcn_readlane(inc, 63);
      if (lane < 16) off[128 + lane] = n;
      const unsigned long long m1 = __ballot(c[1] != 0), m0 = __ballot(c[0] != 0);
      const int l1 = m1 ? 63 - __builtin_clzll(m1) : -1, l0 = m0 ? 63 - __builtin_clzll(m0) : -1;
      glast = max(2 * l1 + 1, 2 * l0); glast = glast < 0 ? 0 : glast;
    }
    const int ntiles = (n + 31) >> 5;
    auto scatter = [&](auto lo_c, auto hi_c) AI {
      constexpr int lo = decltype(lo_c)::value, hi = decltype(hi_c)::value;
      u32 ps[hi - lo];
#pragma unroll
      for (int rr = lo; rr < hi; rr++) {
        const bool v = (vmask >> rr) & 1u;
        const u32 g = v ? (rec[rr][2] >> 16) : 128u;
        ps[rr - lo] = off[g] + rk[rr];
      }
#pragma unroll
      for (int rr = lo; rr < hi; rr++) {
        const bool v = (vmask >> rr) & 1u;
        const u32 p = v ? ps[rr - lo] : (u32)(CAP + (lane & 31));
        u32x2 e; e[0] = rec[rr][0]; e[1] = rec[rr][1];
        *(u32x2*)(lds + L_EA + p * 8) = e;
        *(u32*)(lds + L_SG + (v ? p : p + (u32)SGPAD) * 4) = rec[rr][2];
      }
    };
    scatter(IC<0>{}, IC<8>{});
    if (nrounds > 8) scatter(IC<8>{}, IC<10>{});
    if (nrounds > 10) scatter(IC<10>{}, IC<12>{});
    {
      const int ps = n + lane + 64 * (wv & 3);
      if (wv >= 4 && ps < ntiles * 32 + SGPAD) {
        u32x2 z; z[0] = 0; z[1] = 0;
        if (ps < ntiles * 32) *(u32x2*)(lds + L_EA + ps * 8) = z;
        *(u32*)(lds + L_SG + ps * 4) = 127u << 16;
      }
    }
    BAR();

    int ln = lane;
    asm volatile("" : "+v"(ln));
    const int r = ln & 31, hh = ln >> 5;
    const int G4 = ln >> 4, i16 = ln & 15, tq = i16 >> 2, tp = i16 & 3, blk = G4 & 1;
    const int kk = ln >> 3, c8 = ln & 7;
    u32 gcst[4], aoff[4];
#pragma unroll
    for (int i = 0; i < 4; i++) gcst[i] = (u32)((c8 ^ i ^ (((kk >> 1) & 1) << 2)) * 16);
    {
      const int ii = r & 3, k2 = r >> 2;
#pragma unroll
      for (int s = 0; s < 4; s++)
        aoff[s] = lds0 + (u32)(L_GB + wv * 4096 + (ii * 8 + k2) * 128 + (((4 * hh + s) ^ ii ^ (((k2 >> 1) & 1) << 2)) * 16));
    }
    const int slot = (wv & 3) * 2 + (wv >> 2);
    auto cum = [&](int k) { return (ntiles * ((k >> 1) * 128 + (k & 1) * SPLIT)) >> 9; };
    const int t0 = cum(slot), t1 = cum(slot + 1);
    int head = 127;
    if (t0 < t1) {
      const u32 gb = lds0 + L_GB + wv * 4096;
      const u32 pb = lds0 + L_PB + wv * 1024;
      const u32 d2b = lds0 + L_D2 + wv * 512;
      const u32 a_vw = d2b + r * 8;
      const u32 a_vr = d2b + (4 * hh + tq) * 8;
      const u32 a_z = lds0 + L_ZERO;
      auto sk_read = [&](u32 a_k, u32x4& sk) AI {
        sk[0] = ldl<u32>(a_k); sk[1] = ldl<u32>(a_k + 4); sk[2] = ldl<u32>(a_k + 8); sk[3] = ldl<u32>(a_k + 12);
      };
      auto issue_h = [&](const u32x4& sk) AI {
        glds_idx(gb, sk[0], 128u, gcst[0], h);
        glds_idx(gb + 1024, sk[1], 128u, gcst[1], h);
        glds_idx(gb + 2048, sk[2], 128u, gcst[2], h);
        glds_idx(gb + 3072, sk[3], 128u, gcst[3], h);
      };
      u32 a_sk = lds0 + (u32)(L_SG + (t0 * 32 + 4 * kk) * 4), a_sr = lds0 + (u32)(L_SG + (t0 * 32 + r) * 4);
      { u32x4 sk; sk_read(a_sk, sk); issue_h(sk); a_sk += 128; }
      const int tl = (t0 + ln < t1) ? t0 + ln : t1 - 1;
      const int gfv = (int)(*(const u32*)(lds + L_SG + tl * 128) >> 16);
      const int glv = min((int)(*(const u32*)(lds + L_SG + tl * 128 + 124) >> 16), glast);
      const bool wide = __ballot(glv - gfv >= 8) != 0ull || (t1 - t0) > 64;
      int base = __builtin_amdgcn_readlane(gfv, 0);
      if ((int)__builtin_amdgcn_readfirstlane(off[base]) < t0 * 32) head = base;
      const int gspan = __builtin_amdgcn_readlane(glv, 63);
      f32x16 Y0 = {}, Y1 = {};
      u32 ap[2][2];
      auto load_ap = [&](int bs) AI {
#pragma unroll
        for (int ct = 0; ct < 2; ct++) {
          s16x4 v = trl(lds0 + (u32)(L_AP + (bs + 4 * hh + tq) * 128 + (32 * ct + 16 * blk + 4 * tp) * 2));
          u32x2 u = __builtin_bit_cast(u32x2, v);
          ap[ct][0] = u[0]; ap[ct][1] = u[1];
        }
      };
      u32 pa0, pa1;
      {
        const int b4 = (ln >> 4) & 1, b3 = (ln >> 3) & 1, b2 = (ln >> 2) & 1;
        const int j0 = 4 * b2 + 2 * b3 + b4, j1 = 8 + 2 * b3 + b4;
        pa0 = lds0 + (u32)(L_PACC + ((2 * (j0 / 3) + hh) * 4 + (j0 % 3)) * 4);
        pa1 = lds0 + (u32)(L_PACC + ((2 * (j1 / 3) + hh) * 4 + (j1 % 3)) * 4);
      }
      auto flush = [&](int bs) AI {
        u32 ma[4];
        float o0[4], o1[4];
        const u32 mrow = lds0 + (u32)(L_MACC + hh * (MS * 4) + 4 * r) + (u32)(bs * (MS * 4));
        if (bs + 7 <= gspan && (head == 127 || bs > head)) {
#pragma unroll
          for (int q = 0; q < 4; q++) ma[q] = mrow + (u32)(2 * q * (MS * 4));
        } else {
#pragma unroll
          for (int q = 0; q < 4; q++) {
            const int nd = bs + 2 * q + hh;
            u32 a = mrow + (u32)(2 * q * (MS * 4));
            a = nd > gspan ? lds0 + (u32)(L_MACC + MDUMMY * (MS * 4)) + 4 * r : a;
            a = nd == head ? lds0 + (u32)(L_HB + wv * (MS * 4)) + 4 * r : a;
            ma[q] = a;
          }
        }
#pragma unroll
        for (int q = 0; q < 4; q++) { o0[q] = ldl<float>(ma[q]); o1[q] = ldl<float>(ma[q] + 128); }
        float pr[12];
#pragma unroll
        for (int q = 0; q < 4; q++)
#pragma unroll
          for (int c = 1; c < 4; c++) pr[q * 3 + c - 1] = Y0[4 * q + c] * wx0 + Y1[4 * q + c] * wx1;
        SB();
#pragma unroll
        for (int q = 0; q < 4; q++) { stl<float>(ma[q], o0[q] + Y0[4 * q]); stl<float>(ma[q] + 128, o1[q] + Y1[4 * q]); }
        float x0, x1;
        reduce12(pr, x0, x1);
        masked_add2(pa0 + (u32)(bs * 16), x0, pa1 + (u32)(bs * 16), x1);
        const bf16x8 z8 = BF8((u32x4{0u, 0u, 0u, 0u}));
        Y0 = MFMA(z8, z8, f32x16{});
        Y1 = MFMA(z8, z8, f32x16{});
      };

      if (ALLOW_FAST && !wide) {
        int base1 = base, base2 = base;
        unsigned long long chg = 0;
        {
          int bb = base;
          for (int i = 1; i < t1 - t0; i++) {
            const int gl1 = __builtin_amdgcn_readlane(glv, i);
            if (gl1 >= bb + 8) { bb = __builtin_amdgcn_readlane(gfv, i); chg |= 1ull << i; }
          }
        }
        load_ap(base1);
        u32 bh = (u32)(base1 + 4 * hh);
        f32x16 XA0, XA1, XB0, XB1;
        const u32 a_ps = pb + ln * 16;
        const u32 a_d2w = d2b + ln * 8;
        const u32 st_row = lds0 + (u32)(L_ST + wv * 2048 + r * 64);
        const u32 a_st = lds0 + (u32)(L_ST + wv * 2048 + (4 * hh + tq) * 64 + (4 * blk + tp) * 8);
        u32 a_pd = lds0 + L_PD, a_lut = lds0 + L_LUT;
        PIN(a_pd); PIN(a_lut);
        u32 a_sp = lds0 + (u32)(L_SG + (t0 * 32 + ln) * 4);
        u32 a_x = hh ? d2b + r * 8 : lds0 + (u32)(L_EA + (t0 * 32 + r) * 8);
        const u32 incB = hh ? (u32)-256 : 256u;
        u32 st_prev = st_row, st_g;
        u32x2 vv;
        u32 sgp;
        auto geom = [&](u32 sg, const f32x4& ps, const f32x4& pd) AI {
          const float dx = pd[0] - ps[0], dy = pd[1] - ps[1], dz = pd[2] - ps[2];
          const bool pad = sg >= (127u << 16);
          u32 g0 = pk_bf16(1.0f, dx), g1 = pk_bf16(dy, dz); PIN(g0); PIN(g1);
          vv[0] = pad ? 0u : g0; vv[1] = pad ? 0u : g1;
          u32x2 d; d[0] = pk_bf16(dx * dx + dy * dy + dz * dz, 0.f); d[1] = d[0];
          stl<u32x2>(a_d2w, d);
          st_g = st_row + (sg >> 16) * 8;
        };
        auto st_write = [&](auto half_c, int bs) AI {
          constexpr int HALF = decltype(half_c)::value;
          u32 a = min(st_g - (u32)(bs * 8), st_row + 56u);
          half_write_b64<HALF>(st_prev, a, vv);
        };
        u32 sg_cur;
        {
          sgp = ldl<u32>(a_sp); a_sp += 256;
          glds_idx(pb, sgp, 16u, 0u, P4);
          const u32 sg0 = ldl<u32>(a_sr); a_sr += 128;
          const f32x4 pd = ldl<f32x4>(a_pd + __builtin_amdgcn_ubfe(sgp, 16, 7) * 16);
          const u32 sgn = ldl<u32>(a_sp); a_sp += 256;
          u32x4 sk; sk_read(a_sk, sk); a_sk += 128;
          sg_cur = ldl<u32>(a_sr); a_sr += 128;
          asm volatile("s_waitcnt vmcnt(0)" ::: "memory");
          bf16x8 A[4];
#pragma unroll
          for (int s = 0; s < 4; s++) A[s] = ldl<bf16x8>(aoff[s]);
          const f32x4 ps = ldl<f32x4>(a_ps);
          asm volatile("s_waitcnt lgkmcnt(0)" ::: "memory");
          issue_h(sk);
          geom(sgp, ps, pd);
          sgp = sgn;
          glds_idx(pb, sgp, 16u, 0u, P4);
          st_write(IC<0>{}, base1);
          const u32x2 xx = ldl<u32x2>(a_x); a_x += 256;
          const u32 t4 = min((sg0 >> 16) - bh, 4u);
          const u32x2 oh = ldl<u32x2>(a_lut + t4 * 8);
          XA0 = f32x16{}; XA1 = f32x16{};
#pragma unroll
          for (int s = 0; s < 4; s++) { XA0 = MFMA(A[s], Bw[s][0], XA0); XA1 = MFMA(A[s], Bw[s][1], XA1); }
          u32x4 a4, b0, b1;
          a4[0] = oh[0]; a4[1] = oh[1]; a4[2] = xx[0]; a4[3] = xx[1];
          b0[0] = ap[0][0]; b0[1] = ap[0][1]; b0[2] = xw[0][0]; b0[3] = xw[0][1];
          b1[0] = ap[1][0]; b1[1] = ap[1][1]; b1[2] = xw[1][0]; b1[3] = xw[1][1];
          XA0 = MFMA(BF8(a4), BF8(b0), XA0); XA1 = MFMA(BF8(a4), BF8(b1), XA1);
          asm volatile("s_nop 7\n\ts_nop 7" : "+v"(XA0), "+v"(XA1));
        }
        auto step = [&](auto next_tag, auto par_c, f32x16& C0, f32x16& C1, f32x16& N0, f32x16& N1, int t) AI {
          constexpr bool NEXT = decltype(next_tag)::value;
          constexpr int PAR = decltype(par_c)::value;
          if (NEXT) {
            if (__builtin_expect((chg >> (t + 1 - t0)) & 1ull, 0)) {
              base1 = __builtin_amdgcn_readlane(gfv, t + 1 - t0); load_ap(base1); bh = (u32)(base1 + 4 * hh);
            }
          }
          u32x4 M[2][2];
          bf16x8 A[4]; u32x4 sk; u32 sr, sgn; f32x4 ps, pd;
          u32x2 xx, oh;
          s16x4 sf[4];
          f32x2 tt[16];
          SB();
#define XP(p) (((p) >> 2) & 1 ? __builtin_shufflevector(C1, C1, 8 * ((p) >> 3) + 2 * ((p) & 3), 8 * ((p) >> 3) + 2 * ((p) & 3) + 1) \
                              : __builtin_shufflevector(C0, C0, 8 * ((p) >> 3) + 2 * ((p) & 3), 8 * ((p) >> 3) + 2 * ((p) & 3) + 1))
#define MSET(p, v) M[((p) >> 2) & 1][((p) >> 3) & 1][(p) & 3] = (v)
          static_for<0, 20>([&](auto ic) AI {
            constexpr int k = decltype(ic)::value - 1;
            constexpr bool R = (k - 1 >= 0 && k - 1 < 16), Cv = (k - 3 >= 0 && k - 3 < 16), E = (k + 1 < 16), Ad = (k >= 0 && k < 16), Mu = (k - 2 >= 0 && k - 2 < 16);
            if constexpr (R) {
              f32x2 v; v[0] = __builtin_amdgcn_rcpf(tt[(k - 1) & 15][0]); v[1] = __builtin_amdgcn_rcpf(tt[(k - 1) & 15][1]);
              tt[(k - 1) & 15] = v;
              SB();
            }
            if constexpr (Cv) {
              u32 mo;
              if constexpr (k == 18) asm volatile("v_cvt_pk_bf16_f32 %0, %1, %2\n\ts_nop 1" : "=v"(mo) : "v"(tt[(k - 3) & 15][0]), "v"(tt[(k - 3) & 15][1]));
              else asm volatile("v_cvt_pk_bf16_f32 %0, %1, %2" : "=v"(mo) : "v"(tt[(k - 3) & 15][0]), "v"(tt[(k - 3) & 15][1]));
              MSET((k - 3) & 15, mo);
              SB();
            }
            if constexpr (E) {
              const f32x2 xe = XP((k + 1) & 15);
              f32x2 v; v[0] = __builtin_amdgcn_exp2f(xe[0]); v[1] = __builtin_amdgcn_exp2f(xe[1]);
              tt[(k + 1) & 15] = v;
              SB();
            }
            if constexpr (Ad && Mu) {
              const f32x2 xm = XP((k - 2) & 15);
              asm volatile("v_pk_add_f32 %0, %0, 1.0 op_sel_hi:[1,0]\n\tv_pk_mul_f32 %1, %2, %1" : "+v"(tt[k & 15]), "+v"(tt[(k - 2) & 15]) : "v"(xm));
            } else {
              if constexpr (Ad) asm volatile("v_pk_add_f32 %0, %0, 1.0 op_sel_hi:[1,0]" : "+v"(tt[k & 15]));
              if constexpr (Mu) {
                const f32x2 xm = XP((k - 2) & 15);
                asm volatile("v_pk_mul_f32 %0, %1, %0" : "+v"(tt[(k - 2) & 15]) : "v"(xm));
              }
            }
            SB();
            if constexpr (k == -1) {
#pragma unroll
              for (int u = 0; u < 4; u++) sf[u] = trl(a_st + 512 * u);
              if constexpr (NEXT) {
                if constexpr (PAR == 1) asm volatile("s_waitcnt vmcnt(1)" ::: "memory"); else asm volatile("s_waitcnt vmcnt(0)" ::: "memory");
#pragma unroll
                for (int s = 0; s < 4; s++) A[s] = ldl<bf16x8>(aoff[s]);
                xx = ldl<u32x2>(a_x);
                oh = ldl<u32x2>(a_lut + min((sg_cur >> 16) - bh, 4u) * 8);
                sk_read(a_sk, sk);
                sr = ldl<u32>(a_sr);
              }
            }
            if constexpr (k == 1 && NEXT) {
              asm volatile("s_waitcnt lgkmcnt(0)" ::: "memory");
              issue_h(sk);
              stl<u32x2>(st_prev, u32x2{0u, 0u});
              st_write(IC<1 - PAR>{}, base1);
            }
            if constexpr (NEXT && k >= 2 && k < 12) {
              constexpr int m = k - 2, ks = m >> 1;
              if constexpr ((m & 1) == 0) {
                if constexpr (ks == 0) N0 = MFMA(A[0], Bw[0][0], f32x16{});
                else if constexpr (ks < 4) N0 = MFMA(A[ks & 3], Bw[ks & 3][0], N0);
                else { u32x4 a4, b0; a4[0] = oh[0]; a4[1] = oh[1]; a4[2] = xx[0]; a4[3] = xx[1];
                       b0[0] = ap[0][0]; b0[1] = ap[0][1]; b0[2] = xw[0][0]; b0[3] = xw[0][1]; N0 = MFMA(BF8(a4), BF8(b0), N0); }
              } else {
                if constexpr (ks == 0) N1 = MFMA(A[0], Bw[0][1], f32x16{});
                else if constexpr (ks < 4) N1 = MFMA(A[ks & 3], Bw[ks & 3][1], N1);
                else { u32x4 a4, b1; a4[0] = oh[0]; a4[1] = oh[1]; a4[2] = xx[0]; a4[3] = xx[1];
                       b1[0] = ap[1][0]; b1[1] = ap[1][1]; b1[2] = xw[1][0]; b1[3] = xw[1][1]; N1 = MFMA(BF8(a4), BF8(b1), N1); }
              }
            }
            if constexpr (k == 12) {
              Y0 = MFMA(mk8(sf[0], sf[1]), BF8(M[0][0]), Y0);
              if constexpr (NEXT && PAR == 0) {
                ps = ldl<f32x4>(a_ps);
                pd = ldl<f32x4>(a_pd + __builtin_amdgcn_ubfe(sgp, 16, 7) * 16);
                sgn = ldl<u32>(a_sp);
              }
            }
            if constexpr (k == 13) Y1 = MFMA(mk8(sf[0], sf[1]), BF8(M[1][0]), Y1);
            if constexpr (k == 14 && NEXT && PAR == 0) {
              geom(sgp, ps, pd);
              sgp = sgn; a_sp += 256; PIN(a_sp);
              asm volatile("s_waitcnt lgkmcnt(0)" ::: "memory");
              glds_idx(pb, sgp, 16u, 0u, P4);
            }
            if constexpr (k == 15 && NEXT) {
              sg_cur = sr; a_sk += 128; a_sr += 128; a_x += (PAR == 0 ? incB : 256u); PIN(a_sk); PIN(a_sr); PIN(a_x);
            }
            if constexpr (k == 16) Y0 = MFMA(mk8(sf[2], sf[3]), BF8(M[0][1]), Y0);
            SB();
          });
          Y1 = MFMA(mk8(sf[2], sf[3]), BF8(M[1][1]), Y1);
          SB();
          if (NEXT) { if (__builtin_expect(base1 != base2, 0)) { flush(base2); base2 = base1; } }
        };
        using Tt = std::integral_constant<bool, true>;
        using Ff = std::integral_constant<bool, false>;
        int t = t0;
        while (t + 2 < t1) { step(Tt{}, IC<0>{}, XA0, XA1, XB0, XB1, t); step(Tt{}, IC<1>{}, XB0, XB1, XA0, XA1, t + 1); t += 2; }
        if (t + 1 < t1) { step(Tt{}, IC<0>{}, XA0, XA1, XB0, XB1, t); step(Ff{}, IC<1>{}, XB0, XB1, XA0, XA1, t + 1); }
        else step(Ff{}, IC<0>{}, XA0, XA1, XB0, XB1, t);
        flush(base2);
        asm volatile("s_waitcnt vmcnt(0)" ::: "memory");
      } else {
        u32 slo, slen;
        auto load_sel = [&](int bs) AI {
          const int nl = bs + 4 * blk + tp;
          slo = off[nl]; slen = off[nl + 1] - slo;
        };
        const u32 a_ps = pb + r * 16;
        load_ap(base); load_sel(base);
        u32 sg_cur = ldl<u32>(a_sr); a_sr += 128;
        glds_idx_lo32(pb, sg_cur, P4);
        for (int t = t0; t < t1; t++) {
          const int Tb = t * 32;
          const u32x2 ea = *(const u32x2*)(lds + L_EA + (Tb + r) * 8);
          const f32x4 pd = *(const f32x4*)(lds + L_PD + ((sg_cur >> 16) & 127u) * 16);
          u32x4 sk; sk_read(a_sk, sk);
          const u32 sr = ldl<u32>(a_sr);
          asm volatile("s_waitcnt vmcnt(0)" ::: "memory");
          bf16x8 A[4];
#pragma unroll
          for (int s = 0; s < 4; s++) A[s] = ldl<bf16x8>(aoff[s]);
          const f32x4 ps = ldl<f32x4>(a_ps);
          asm volatile("s_waitcnt lgkmcnt(0)" ::: "memory");
          issue_h(sk);
          glds_idx_lo32(pb, sr, P4);
          const u32 sg = sg_cur;
          const float dx = pd[0] - ps[0], dy = pd[1] - ps[1], dz = pd[2] - ps[2];
          { u32x2 v; v[0] = pk_bf16(1.0f, dx); v[1] = pk_bf16(dy, dz); stl<u32x2>(a_vw, v); }
          const u32 d2 = pk_bf16(dx * dx + dy * dy + dz * dz, 0.f);
          sg_cur = sr; a_sk += 128; a_sr += 128;
          const int g = min((int)(sg >> 16), glast);
          const int gf = __builtin_amdgcn_readlane(g, 0), gl = __builtin_amdgcn_readlane(g, 31);
          if (gl >= base + 8) { flush(base); base = gf; load_ap(base); load_sel(base); }
          f32x16 X0 = {}, X1 = {};
#pragma unroll
          for (int s = 0; s < 4; s++) { X0 = MFMA(A[s], Bw[s][0], X0); X1 = MFMA(A[s], Bw[s][1], X1); }
          for (int wb = base;; wb += 8) {
            if (wb != base) load_ap(wb);
            const u32 t4 = (u32)g - (u32)(wb + 4 * hh);
            unsigned long long oh = 0x3F80ull << ((t4 & 3u) * 16u);
            if (t4 >= 4u) oh = 0ull;
            u32x4 a4; a4[0] = (u32)oh; a4[1] = (u32)(oh >> 32);
            a4[2] = wb == base ? (hh ? d2 : ea[0]) : 0u; a4[3] = wb == base ? (hh ? d2 : ea[1]) : 0u;
            u32x4 b0, b1;
            b0[0] = ap[0][0]; b0[1] = ap[0][1]; b0[2] = xw[0][0]; b0[3] = xw[0][1];
            b1[0] = ap[1][0]; b1[1] = ap[1][1]; b1[2] = xw[1][0]; b1[3] = xw[1][1];
            X0 = MFMA(BF8(a4), BF8(b0), X0);
            X1 = MFMA(BF8(a4), BF8(b1), X1);
            if (gl < wb + 8) { if (wb != base) load_ap(base); break; }
          }
          u32x4 M[2][2];
#pragma unroll
          for (int s = 0; s < 2; s++)
#pragma unroll
            for (int j = 0; j < 4; j++) {
              M[0][s][j] = silu2(X0[8 * s + 2 * j], X0[8 * s + 2 * j + 1]);
              M[1][s][j] = silu2(X1[8 * s + 2 * j], X1[8 * s + 2 * j + 1]);
            }
          for (;;) {
            s16x4 sf[4];
            const u32 d0 = (u32)(Tb + 4 * hh + tq) - slo;
#pragma unroll
            for (int u = 0; u < 4; u++) sf[u] = trl(((d0 + 8u * u) < slen ? a_vr : a_z) + 64 * u);
#pragma unroll
            for (int s = 0; s < 2; s++) {
              const bf16x8 S = mk8(sf[2 * s], sf[2 * s + 1]);
              Y0 = MFMA(S, BF8(M[0][s]), Y0);
              Y1 = MFMA(S, BF8(M[1][s]), Y1);
            }
            if (gl < base + 8) break;
            flush(base); base += 8; load_ap(base); load_sel(base);
          }
        }
        flush(base);
        asm volatile("s_waitcnt vmcnt(0)" ::: "memory");
      }
    }
    if (ALLOW_FAST) {
      glds16(lds0 + L_GB + wv * 4096, (u32)(wv * 2048 + lane * 16), wh);
      glds16(lds0 + L_GB + wv * 4096 + 1024, (u32)(wv * 2048 + 1024 + lane * 16), wh);
    }
    if (lane == 0) misc[16 + slot] = (u32)head;
    BAR();
    {
      float* hb = (float*)(lds + L_HB) + wv * MS;
      const int hprev = slot > 0 ? (int)misc[16 + slot - 1] : 127, hnext = slot < 7 ? (int)misc[16 + slot + 1] : 127;
      const bool uniq = (hprev != head) && (hnext != head);
      if (head != 127) {
        const float v = hb[lane];
        hb[lane] = 0.f;
        if (uniq) macc[head * MS + lane] += v;
        else atomicAdd(&macc[head * MS + lane], v);
      }
      if (tid < 144) hist[tid] = 0;
    }
  };

  load_records(0, 4 * MAXR);
  int Pmax = 0, ntot = 0;
  {
    for (int i = tid; i < (99 + 8) * MS / 4; i += 512) *(f32x4*)(macc + 4 * i) = f32x4{0.f, 0.f, 0.f, 0.f};
    if (tid < 105) *(f32x4*)(pacc + 4 * tid) = f32x4{0.f, 0.f, 0.f, 0.f};
    if (tid < 64) *(u32*)(lds + L_ZERO + tid * 4) = 0;
    if (tid < 5) { u32x2 v; v[0] = tid == 0 ? 0x3F80u : (tid == 1 ? 0x3F800000u : 0u); v[1] = tid == 2 ? 0x3F80u : (tid == 3 ? 0x3F800000u : 0u); *(u32x2*)(lds + L_LUT + tid * 8) = v; }
#pragma unroll
    for (int i = 0; i < 2; i++) *(u32x4*)(lds + L_ST + (tid + 512 * i) * 16) = u32x4{0, 0, 0, 0};
    if (tid < 128) { f32x4 v; v[0] = bf2f(pd0); v[1] = bf2f(pd1); v[2] = bf2f(pd2); v[3] = 0.f; *(f32x4*)(lds + L_PD + tid * 16) = v; }
    if (tid < 144) hist[tid] = 0;
    if (lane == 0) { misc[wv] = (u32)P; misc[8 + wv] = (u32)csum; }
    u32x4 o;
#pragma unroll
    for (int i = 0; i < 4; i++) o[i] = pk_bf16(bflo(wB[i]) * NLOG2E, bfhi(wB[i]) * NLOG2E);
    const u32x4 z4 = {0, 0, 0, 0};
    *(u32x4*)(lds + L_WE + tid * 16) = wA;
    *(u32x4*)(lds + L_WE + 8192 + tid * 16) = o;
    if (tid < 40) *(u32x4*)(lds + L_WE + 16384 + tid * 16) = wC;
    *(u32x4*)(lds + L_H2 + tid * 16) = tid < nn * 8 ? hA : z4;
    if (tid < 288) *(u32x4*)(lds + L_H2 + 8192 + tid * 16) = tid + 512 < nn * 8 ? hB : z4;
    BAR();
#pragma unroll
    for (int i = 0; i < 8; i++) { Pmax = max(Pmax, (int)misc[i]); ntot += (int)misc[8 + i]; }
    Pmax = __builtin_amdgcn_readfirstlane(Pmax); ntot = __builtin_amdgcn_readfirstlane(ntot);
#pragma unroll
    for (int s = 0; s < 4; s++)
#pragma unroll
      for (int ct = 0; ct < 2; ct++) {
        const u32 p = lds0 + (u32)(L_WE + (64 + 32 * hh + 8 * s + tq) * 128 + (32 * ct + 16 * blk + 4 * tp) * 2);
        Bw[s][ct] = mk8(trl(p), trl(p + 4 * 128));
      }
#pragma unroll
    for (int ct = 0; ct < 2; ct++) {
      const u16* wt = (const u16*)(lds + L_WE + 16384);
      const int col = 32 * ct + r;
      const float w0 = bf2f(wt[col]) * NLOG2E, w1 = bf2f(wt[64 + col]) * NLOG2E, w2 = bf2f(wt[128 + col]) * NLOG2E,
                  w3 = bf2f(wt[192 + col]) * NLOG2E, w4 = bf2f(wt[256 + col]) * NLOG2E;
      const u32 hi = pk_bf16(w0, 0.f) & 0xffffu;
      const u32 lo = pk_bf16(w0 - bf2f(hi), 0.f) & 0xffffu;
      xw[ct][0] = hh ? hi : pk_bf16(w1, w2);
      xw[ct][1] = hh ? lo : pk_bf16(w3, w4);
    }
    {
      const int nt = wv >> 1, ct = wv & 1;
      f32x16 acc = {};
#pragma unroll
      for (int s = 0; s < 4; s++) {
        const u32 p = lds0 + (u32)(L_WE + (32 * hh + 8 * s + tq) * 128 + (32 * ct + 16 * blk + 4 * tp) * 2);
        bf16x8 aw = mk8(trl(p), trl(p + 4 * 128));
        bf16x8 bh = *(const bf16x8*)(lds + L_H2 + min(32 * nt + r, 99) * 128 + (4 * hh + s) * 16);
        acc = MFMA(aw, bh, acc);
      }
      const int node = 32 * nt + r;
      char* ap = lds + (node < 105 ? L_AP + node * 128 : L_DESC + 2048 + wv * 128) + (32 * ct + 4 * hh) * 2;
#pragma unroll
      for (int q = 0; q < 4; q++) {
        u32x2 v;
        v[0] = pk_bf16(acc[4 * q] * NLOG2E, acc[4 * q + 1] * NLOG2E);
        v[1] = pk_bf16(acc[4 * q + 2] * NLOG2E, acc[4 * q + 3] * NLOG2E);
        *(u32x2*)(ap + 16 * q) = v;
      }
    }
  }
  if (Pmax <= 4 * MAXR && ntot <= CAP) {
    process(std::integral_constant<bool, true>{});
  } else {
    for (int plo = 0; plo < Pmax; plo += FPPB) {
      BAR();
      load_records(plo, FPPB);
      process(std::integral_constant<bool, false>{});
    }
  }

  if (!(Pmax <= 4 * MAXR && ntot <= CAP)) {
    glds16(lds0 + L_GB + wv * 4096, (u32)(wv * 2048 + lane * 16), wh);
    glds16(lds0 + L_GB + wv * 4096 + 1024, (u32)(wv * 2048 + 1024 + lane * 16), wh);
  }
  asm volatile("s_waitcnt vmcnt(0)" ::: "memory");
  BAR();
  {
    int ln = lane;
    asm volatile("" : "+v"(ln));
    const int r = ln & 31, hh = ln >> 5;
    const int G4 = ln >> 4, i16 = ln & 15, tq = i16 >> 2, tp = i16 & 3, blk = G4 & 1;
    const int nt = wv >> 1, ct = wv & 1;
    const int node = 32 * nt + r;
    const int nrow = node < NB ? node : MDUMMY;
    f32x16 acc = {};
#pragma unroll
    for (int s = 0; s < 8; s++) {
      const int kb = (s < 4) ? (32 * hh + 8 * s) : (64 + 32 * hh + 8 * (s - 4));
      const int k0 = kb + tq;
      const u32 p = lds0 + (u32)(L_GB + (k0 >> 4) * 4096 + (k0 & 15) * 128 + (32 * ct + 16 * blk + 4 * tp) * 2);
      const bf16x8 aw = mk8(trl(p), trl(p + 4 * 128));
      bf16x8 bb;
      if (s < 4) {
        bb = *(const bf16x8*)(lds + L_H2 + min(node, 99) * 128 + (4 * hh + s) * 16);
      } else {
        const float* mp = macc + nrow * MS + 32 * hh + 8 * (s - 4);
        const f32x4 m0 = *(const f32x4*)mp, m1 = *(const f32x4*)(mp + 4);
        u32x4 pk;
        pk[0] = pk_bf16(m0[0] * NLN2, m0[1] * NLN2); pk[1] = pk_bf16(m0[2] * NLN2, m0[3] * NLN2);
        pk[2] = pk_bf16(m1[0] * NLN2, m1[1] * NLN2); pk[3] = pk_bf16(m1[2] * NLN2, m1[3] * NLN2);
        bb = __builtin_bit_cast(bf16x8, pk);
      }
      acc = MFMA(aw, bb, acc);
    }
#pragma unroll
    for (int q = 0; q < 4; q++) {
      float u[4];
#pragma unroll
      for (int i = 0; i < 4; i++) {
        const float x = bf2f(pk_bf16(acc[4 * q + i], 0.f) & 0xffffu);
        u[i] = x * __builtin_amdgcn_rcpf(1.0f + __builtin_amdgcn_exp2f(x * NLOG2E));
      }
      u32x2 v; v[0] = pk_bf16(u[0], u[1]); v[1] = pk_bf16(u[2], u[3]);
      *(u32x2*)(lds + L_UT + node * 144 + (32 * ct + 8 * q + 4 * hh) * 2) = v;
    }
  }
  BAR();
#pragma unroll
  for (int k = 0; k < 2; k++) {
    const int i = tid + 512 * k;
    if (i < nn * 8) {
      const int row = i >> 3, chn = i & 7;
      const u32x4 u = *(const u32x4*)(lds + L_UT + row * 144 + chn * 16);
      const u32x4 hv = *(const u32x4*)(lds + L_H2 + i * 16);
      u32x4 o;
#pragma unroll
      for (int j = 0; j < 4; j++) o[j] = pk_bf16(bflo(hv[j]) + bflo(u[j]), bfhi(hv[j]) + bfhi(u[j]));
      *(u32x4*)(out + (size_t)(node0 + row) * 64 + chn * 8) = o;
    }
  }
  if (tid < nn * 3) {
    const int nd = tid / 3, c = tid - nd * 3;
    const float pv = *(const float*)(lds + L_PD + nd * 16 + c * 4);
    const float dlt = bf2f(pk_bf16(pacc[nd * 4 + c], 0.f) & 0xffffu);
    out[(size_t)N * 64 + (size_t)node0 * 3 + tid] = (u16)(pk_bf16(pv + dlt, 0.f) & 0xffffu);
  }
}

extern "C" void kernel_launch(void* const* d_in, const int* in_sizes, int n_in, void* d_out, int out_size, void* d_ws,
                              size_t ws_size, hipStream_t stream) {
  const u16* h = (const u16*)d_in[0];
  const u16* pos = (const u16*)d_in[1];
  const u16* ea = (const u16*)d_in[2];
  const int* src = (const int*)d_in[3];
  const int* dst = (const int*)d_in[4];
  const u16* we = (const u16*)d_in[5];
  const u16* wx = (const u16*)d_in[6];
  const u16* wh = (const u16*)d_in[7];
  const int N = in_sizes[0] / 64, E = in_sizes[3];
  char* ws = (char*)d_ws;
  u32* R = (u32*)ws;
  f32x4* P4 = (f32x4*)(ws + (size_t)10 * 1048576);
  u32* T = (u32*)(ws + (size_t)11 * 1048576);
  (void)hipFuncSetAttribute((const void*)k1_partition, hipFuncAttributeMaxDynamicSharedMemorySize, K1_LDS);
  (void)hipFuncSetAttribute((const void*)k2_main, hipFuncAttributeMaxDynamicSharedMemorySize, K2_LDS);
  k1_partition<<<(E + CH - 1) / CH, 1024, K1_LDS, stream>>>(pos, (const u32x4*)ea, (const i32x4*)src, (const i32x4*)dst, R, P4, T, N, E);
  k2_main<<<(N + NB - 1) / NB, 512, K2_LDS, stream>>>(h, pos, we, wx, wh, R, P4, T, (u16*)d_out, N);
}
```

```cpp
#include <hip/hip_runtime.h>
#include <stdint.h>
#include <type_traits>

typedef unsigned short u16;
typedef unsigned char u8;
typedef unsigned int u32;
typedef short s16x4 __attribute__((ext_vector_type(4)));
typedef __bf16 bf16x2 __attribute__((ext_vector_type(2)));
typedef __bf16 bf16x8 __attribute__((ext_vector_type(8)));
typedef float f32x2 __attribute__((ext_vector_type(2)));
typedef float f32x4 __attribute__((ext_vector_type(4)));
typedef float f32x16 __attribute__((ext_vector_type(16)));
typedef u32 u32x2 __attribute__((ext_vector_type(2)));
typedef u32 u32x3 __attribute__((ext_vector_type(3)));
typedef u32 u32x4 __attribute__((ext_vector_type(4)));
typedef int i32x4 __attribute__((ext_vector_type(4)));
#define LAS __attribute__((address_space(3)))
#define DI __device__ __forceinline__
#define AI __attribute__((always_inline))

constexpr int NB    = 98;
constexpr int CH    = 3128;
constexpr int NCH   = 256;
constexpr int CAP   = 3392;
constexpr int MAXR  = 12;
constexpr int FPPB  = 26;
#define SPLIT 86
constexpr float NLOG2E = -1.4426950408889634f;
constexpr float NLN2   = -0.6931471805599453f;

DI float bf2f(u32 bits16) { return __uint_as_float(bits16 << 16); }
DI float bflo(u32 v) { return __uint_as_float(v << 16); }
DI float bfhi(u32 v) { return __uint_as_float(v & 0xffff0000u); }
DI u32 pk_bf16(float lo, float hi) {
  f32x2 v; v[0] = lo; v[1] = hi;
  return __builtin_bit_cast(u32, __builtin_convertvector(v, bf16x2));
}
template <int CTRL, int RMASK> DI int dpp_i(int x) { return __builtin_amdgcn_update_dpp(0, x, CTRL, RMASK, 0xf, false); }
template <int CTRL> DI float dpp_f(float x) {
  return __builtin_bit_cast(float, __builtin_amdgcn_update_dpp(0, __builtin_bit_cast(int, x), CTRL, 0xf, 0xf, true));
}
DI int wave_incl_scan(int x) {
  x += dpp_i<0x111, 0xf>(x); x += dpp_i<0x112, 0xf>(x); x += dpp_i<0x114, 0xf>(x); x += dpp_i<0x118, 0xf>(x);
  x += dpp_i<0x142, 0xa>(x); x += dpp_i<0x143, 0xc>(x);
  return x;
}
DI float half_reduce(float x) {
  x += dpp_f<0x111>(x); x += dpp_f<0x112>(x); x += dpp_f<0x114>(x); x += dpp_f<0x118>(x);
  x += dpp_f<0x142>(x);
  return x;
}
DI void glds16(u32 lds_addr, u32 voff, const void* base) {
  asm volatile("s_mov_b32 m0, %0\n\ts_nop 0\n\tglobal_load_lds_dwordx4 %1, %2" ::"s"(lds_addr), "v"(voff), "s"(base) : "memory");
}
DI void glds16_lo32(u32 lds_addr, u32 voff, const void* base) {
  asm volatile("s_mov_b32 m0, %0\n\ts_mov_b64 exec, 0xffffffff\n\tglobal_load_lds_dwordx4 %1, %2\n\ts_mov_b64 exec, -1"
               ::"s"(lds_addr), "v"(voff), "s"(base) : "memory");
}
DI void glds_idx(u32 lds_addr, u32 idx, u32 mul, u32 add, const void* base) {
  u32 t;
  asm volatile("s_mov_b32 m0, %1\n\tv_mad_u32_u16 %0, %2, %3, %4\n\tglobal_load_lds_dwordx4 %0, %5"
               : "=&v"(t) : "s"(lds_addr), "v"(idx), "s"(mul), "v"(add), "s"(base) : "memory");
}
DI void glds_idx_lo32(u32 lds_addr, u32 idx, const void* base) {
  u32 t;
  asm volatile("s_mov_b32 m0, %1\n\tv_mad_u32_u16 %0, %2, 16, 0\n\ts_mov_b64 exec, 0xffffffff\n\tglobal_load_lds_dwordx4 %0, %3\n\ts_mov_b64 exec, -1"
               : "=&v"(t) : "s"(lds_addr), "v"(idx), "s"(base) : "memory");
}
DI u32 mad16(u32 a_lo16, u32 b, u32 c) {
  u32 d;
  asm("v_mad_u32_u16 %0, %1, %2, %3" : "=v"(d) : "v"(a_lo16), "s"(b), "v"(c));
  return d;
}
template <int HALF> DI void half_write_b64(u32& prev, u32 a, u32x2 v) {
  if constexpr (HALF == 0)
    asm volatile("s_mov_b64 exec, 0xffffffff\n\tds_write_b64 %1, %2\n\tv_mov_b32 %0, %1\n\ts_mov_b64 exec, -1" : "+v"(prev) : "v"(a), "v"(v) : "memory");
  else
    asm volatile("s_mov_b64 exec, %3\n\tds_write_b64 %1, %2\n\tv_mov_b32 %0, %1\n\ts_mov_b64 exec, -1" : "+v"(prev) : "v"(a), "v"(v), "s"(0xffffffff00000000ull) : "memory");
}
DI void reduce12(float (&v)[12], float& x0, float& x1) {
  asm volatile(
      "s_nop 1\n\t"
      "v_permlane16_swap_b32 %0, %1\n\tv_permlane16_swap_b32 %2, %3\n\tv_permlane16_swap_b32 %4, %5\n\t"
      "v_permlane16_swap_b32 %6, %7\n\tv_permlane16_swap_b32 %8, %9\n\tv_permlane16_swap_b32 %10, %11\n\t"
      "s_nop 1\n\t"
      "v_add_f32 %0, %0, %1\n\tv_add_f32 %2, %2, %3\n\tv_add_f32 %4, %4, %5\n\t"
      "v_add_f32 %6, %6, %7\n\tv_add_f32 %8, %8, %9\n\tv_add_f32 %10, %10, %11\n\t"
      "v_add_f32_dpp %1, %0, %0 row_ror:8 row_mask:0xf bank_mask:0x3\n\t"
      "v_add_f32_dpp %3, %4, %4 row_ror:8 row_mask:0xf bank_mask:0x3\n\t"
      "v_add_f32_dpp %5, %8, %8 row_ror:8 row_mask:0xf bank_mask:0x3\n\t"
      "v_add_f32_dpp %1, %2, %2 row_ror:8 row_mask:0xf bank_mask:0xc\n\t"
      "v_add_f32_dpp %3, %6, %6 row_ror:8 row_mask:0xf bank_mask:0xc\n\t"
      "v_add_f32_dpp %5, %10, %10 row_ror:8 row_mask:0xf bank_mask:0xc\n\t"
      "v_add_f32_dpp %7, %1, %1 row_half_mirror row_mask:0xf bank_mask:0x5\n\t"
      "v_add_f32_dpp %7, %3, %3 row_half_mirror row_mask:0xf bank_mask:0xa\n\t"
      "v_add_f32_dpp %9, %5, %5 row_half_mirror row_mask:0xf bank_mask:0xf\n\t"
      "s_nop 1\n\t"
      "v_add_f32_dpp %7, %7, %7 quad_perm:[1,0,3,2] row_mask:0xf bank_mask:0xf\n\t"
      "v_add_f32_dpp %9, %9, %9 quad_perm:[1,0,3,2] row_mask:0xf bank_mask:0xf\n\t"
      "s_nop 1\n\t"
      "v_add_f32_dpp %7, %7, %7 quad_perm:[2,3,0,1] row_mask:0xf bank_mask:0xf\n\t"
      "v_add_f32_dpp %9, %9, %9 quad_perm:[2,3,0,1] row_mask:0xf bank_mask:0xf\n\t"
      "s_nop 0"
      : "+v"(v[0]), "+v"(v[1]), "+v"(v[2]), "+v"(v[3]), "+v"(v[4]), "+v"(v[5]), "+v"(v[6]), "+v"(v[7]), "+v"(v[8]), "+v"(v[9]),
        "+v"(v[10]), "+v"(v[11]));
  x0 = v[7]; x1 = v[9];
}
DI void masked_add2(u32 a0, float x0, u32 a1, float x1) {
  asm volatile("s_mov_b64 exec, %4\n\tds_add_f32 %0, %1\n\ts_mov_b64 exec, %5\n\tds_add_f32 %2, %3\n\ts_mov_b64 exec, -1"
               ::"v"(a0), "v"(x0), "v"(a1), "v"(x1), "s"(0x1111111111111111ull), "s"(0x0101010101010101ull) : "memory");
}
template <class T> DI T ldl(u32 a) { return *(const LAS T*)(uintptr_t)a; }
template <class T> DI void stl(u32 a, T v) { *(LAS T*)(uintptr_t)a = v; }
DI s16x4 trl(u32 a) { return __builtin_amdgcn_ds_read_tr16_b64_v4i16((LAS s16x4*)(uintptr_t)a); }
DI bf16x8 mk8(s16x4 lo, s16x4 hi) {
  return __builtin_bit_cast(bf16x8, __builtin_shufflevector(lo, hi, 0, 1, 2, 3, 4, 5, 6, 7));
}
#define BAR() do { asm volatile("s_waitcnt lgkmcnt(0)" ::: "memory"); __builtin_amdgcn_s_barrier(); asm volatile("" ::: "memory"); } while (0)
#define SB() __builtin_amdgcn_sched_barrier(0)
#define PIN(x) asm volatile("" : "+v"(x))
template <int I, int N, class F> DI void static_for(F&& f) {
  if constexpr (I < N) { f(std::integral_constant<int, I>{}); static_for<I + 1, N>(f); }
}
template <int I> using IC = std::integral_constant<int, I>;
#define MFMA(a, b, c) __builtin_amdgcn_mfma_f32_32x32x16_bf16((a), (b), (c), 0, 0, 0)
#define BF8(x) __builtin_bit_cast(bf16x8, (x))

constexpr int K1_HIST = CH * 12 + 64;
constexpr int K1_OFF = K1_HIST + 1024 + 64;
constexpr int K1_LDS = K1_OFF + 1024 + 64;

__global__ __launch_bounds__(1024) void k1_partition(const u16* __restrict__ pos, const u32x4* __restrict__ ea4,
                                                     const i32x4* __restrict__ src4, const i32x4* __restrict__ dst4,
                                                     u32* __restrict__ R, f32x4* __restrict__ P4, u32* __restrict__ T,
                                                     int N, int E) {
  extern __shared__ __attribute__((aligned(16))) char lds[];
  const int tid = threadIdx.x, lane = tid & 63;
  const int wv = __builtin_amdgcn_readfirstlane(tid >> 6);
  const int w = blockIdx.x;
  u32* hist = (u32*)(lds + K1_HIST);
  u32* off = (u32*)(lds + K1_OFF);
  int cnt = E - w * CH; cnt = cnt > CH ? CH : cnt; cnt = cnt < 4 ? 4 : cnt;
  const bool act = 4 * tid < cnt;
  const int q = (w * CH >> 2) + (act ? tid : 0);
  const i32x4 d4 = __builtin_nontemporal_load(&dst4[q]);
  const i32x4 s4 = __builtin_nontemporal_load(&src4[q]);
  const u32x4 a0 = __builtin_nontemporal_load(&ea4[2 * q]);
  const u32x4 a1 = __builtin_nontemporal_load(&ea4[2 * q + 1]);
  const int pn = w * NB + (tid - 896);
  const bool pjob = tid >= 896 && tid < 896 + NB && pn < N;
  const u16* pp = pos + (size_t)(pjob ? pn : 0) * 3;
  const u32 pj0 = pp[0], pj1 = pp[1], pj2 = pp[2];
  if (tid < 272) hist[tid] = 0;
  BAR();
  int bk[4], gl[4], rk[4];
#pragma unroll
  for (int i = 0; i < 4; i++) {
    const u32 d = (u32)d4[i];
    bk[i] = (int)((d * 42800u) >> 22);
    gl[i] = (int)d - bk[i] * NB;
    bk[i] = act ? bk[i] : 256 + (lane & 15);
    rk[i] = atomicAdd(&hist[bk[i]], 1u);
  }
  BAR();
  {
    u32x4 c = *(const u32x4*)(hist + 4 * lane);
    int s = c[0] + c[1] + c[2] + c[3];
    int inc = wave_incl_scan(s);
    u32x4 o;
    o[0] = inc - s; o[1] = o[0] + c[0]; o[2] = o[1] + c[1]; o[3] = o[2] + c[2];
    *(u32x4*)(off + 4 * lane) = o;
    if (wv == 15) {
#pragma unroll
      for (int i = 0; i < 4; i++) T[(4 * lane + i) * NCH + w] = o[i] | (c[i] << 16);
    }
  }
  if (act) {
#pragma unroll
    for (int i = 0; i < 4; i++) {
      const int slot = (int)(off[bk[i]] + rk[i]);
      u32* p = (u32*)(lds + slot * 12);
      p[0] = i < 2 ? a0[2 * i] : a1[2 * i - 4];
      p[1] = i < 2 ? a0[2 * i + 1] : a1[2 * i - 3];
      p[2] = (u32)s4[i] | ((u32)gl[i] << 16);
    }
  }
  BAR();
  u32x4* Rw = (u32x4*)(R + (size_t)w * CH * 3);
  const int n16 = (cnt * 12 + 15) >> 4;
#pragma unroll
  for (int k = 0; k < 3; k++) {
    const int i = tid + 1024 * k;
    if (i < n16) __builtin_nontemporal_store(*(const u32x4*)(lds + i * 16), &Rw[i]);
  }
  if (pjob) { f32x4 v; v[0] = bf2f(pj0); v[1] = bf2f(pj1); v[2] = bf2f(pj2); v[3] = 0.f; P4[pn] = v; }
}

constexpr int L_GB = 0;
constexpr int L_PB = L_GB + 8 * 4096;
constexpr int L_D2 = L_PB + 8 * 1024;
constexpr int L_ST = L_D2 + 8 * 512;
constexpr int L_EA = L_ST + 8 * 2048;
constexpr int L_SG = L_EA + (CAP + 32) * 8;
constexpr int SGPAD = 128;
constexpr int L_AP = L_SG + (CAP + 160) * 4;
constexpr int MS = 68;
constexpr int MDUMMY = 98;
constexpr int L_MACC = L_AP + 105 * 128;
constexpr int L_HB = L_MACC + 99 * MS * 4;
constexpr int L_PACC = L_HB + 8 * MS * 4;
constexpr int L_HIST = L_PACC + 105 * 16;
constexpr int L_OFF = L_HIST + 144 * 4;
constexpr int L_ZERO = L_OFF + 144 * 4;
constexpr int L_LUT = L_ZERO + 256;
constexpr int L_PD = L_LUT + 64;
constexpr int L_MISC = L_PD + 128 * 16;
constexpr int L_H2 = L_MISC + 256;
constexpr int K2_LDS = L_H2 + 12800;
static_assert(K2_LDS <= 163840, "LDS");
static_assert(L_AP % 16 == 0 && L_MACC % 16 == 0 && L_EA % 16 == 0 && L_SG % 16 == 0 && L_HB % 16 == 0 && L_PD % 16 == 0 && L_H2 % 16 == 0, "align");
constexpr int L_WE = L_EA;
constexpr int L_DESC = L_GB;
constexpr int L_UT = L_EA;
constexpr int L_WH = L_EA + 128 * 144;

DI u32 silu2(float z0, float z1) {
  f32x2 z; z[0] = z0; z[1] = z1;
  f32x2 t; t[0] = __builtin_amdgcn_exp2f(z0); t[1] = __builtin_amdgcn_exp2f(z1);
  t = t + 1.0f;
  f32x2 r; r[0] = __builtin_amdgcn_rcpf(t[0]); r[1] = __builtin_amdgcn_rcpf(t[1]);
  f32x2 m = z * r;
  return __builtin_bit_cast(u32, __builtin_convertvector(m, bf16x2));
}

__global__ __launch_bounds__(512) void k2_main(const u16* __restrict__ h, const u16* __restrict__ pos,
                                               const u16* __restrict__ we, const u16* __restrict__ wx,
                                               const u16* __restrict__ wh, const u32* __restrict__ R,
                                               const f32x4* __restrict__ P4, const u32* __restrict__ T,
                                               u16* __restrict__ out, int N) {
  extern __shared__ __attribute__((aligned(16))) char lds[];
  const int tid = threadIdx.x, lane = tid & 63;
  const int wv = __builtin_amdgcn_readfirstlane(tid >> 6);
  const int b = blockIdx.x;
  const int node0 = b * NB;
  int nn = N - node0; nn = nn > NB ? NB : nn; nn = nn < 1 ? 1 : nn;
  const int r = lane & 31, hh = lane >> 5;
  const u32 lds0 = (u32)(uintptr_t)(LAS char*)lds;
  u32* hist = (u32*)(lds + L_HIST);
  u32* off = (u32*)(lds + L_OFF);
  float* macc = (float*)(lds + L_MACC);
  float* pacc = (float*)(lds + L_PACC);
  u32* misc = (u32*)(lds + L_MISC);

  const u32 tv = T[b * NCH + wv * 32 + r];
  const u32x4* we16 = (const u32x4*)we;
  const u32x4 wA = we16[tid], wB = we16[512 + tid];
  const u32x4 wC = we16[1024 + (tid < 40 ? tid : 0)];
  const u32x4* h16 = (const u32x4*)(h + (size_t)node0 * 64);
  const u32x4 hA = h16[tid < nn * 8 ? tid : 0];
  const u32x4 hB = h16[tid + 512 < nn * 8 ? tid + 512 : 0];
  const u32x4* wh16 = (const u32x4*)wh;
  const u16* pp = pos + (size_t)(node0 + (tid < nn ? tid : 0)) * 3;
  const u32 pd0 = pp[0], pd1 = pp[1], pd2 = pp[2];
  const float wx0 = bf2f(wx[r]) * NLN2, wx1 = bf2f(wx[32 + r]) * NLN2;

  const u32 cstart = tv & 0xffffu;
  const u32 ccnt = lane < 32 ? (tv >> 16) : 0u;
  const int np = (int)((ccnt + 15) >> 4);
  const int pinc = wave_incl_scan(np);
  const int pexc = pinc - np;
  const int P = __builtin_amdgcn_readlane(pinc, 31);
  const int csum = __builtin_amdgcn_readlane(wave_incl_scan((int)ccnt), 31);

  const int G4 = lane >> 4, i16 = lane & 15, tq = i16 >> 2, tp = i16 & 3, blk = G4 & 1;
  bf16x8 Bw[4][2];
  u32 xw[2][2];
  u32x3 rec[MAXR];
  u32 vmask = 0;
  int nrounds = 0;
#pragma unroll
  for (int rr = 8; rr < MAXR; rr++) rec[rr] = u32x3{0, 0, 0};
  u32* desc = (u32*)(lds + L_DESC + wv * 256);
  auto load_records = [&](int plo, int ppb) AI {
    int k0 = plo - pexc; k0 = k0 < 0 ? 0 : k0;
    int k1 = plo + ppb - pexc; k1 = k1 > np ? np : k1;
    const u32 gbase = (u32)((wv * 32 + r) * CH) + cstart;
    for (int k = k0; k < k1; k++) {
      u32 nrec = ccnt - 16 * k; nrec = nrec > 16 ? 16 : nrec;
      desc[pexc + k - plo] = (gbase + 16 * k) | (nrec << 24);
    }
    int Pb = P - plo; Pb = Pb < 0 ? 0 : Pb; Pb = Pb > ppb ? ppb : Pb;
    nrounds = (Pb + 3) >> 2;
    vmask = 0;
    auto rounds = [&](auto lo_c, auto hi_c) AI {
      constexpr int lo = decltype(lo_c)::value, hi = decltype(hi_c)::value;
      u32 dd[hi - lo];
#pragma unroll
      for (int rr = lo; rr < hi; rr++) dd[rr - lo] = desc[4 * rr + G4];
#pragma unroll
      for (int rr = lo; rr < hi; rr++) {
        const u32 d = (4 * rr + G4 < Pb) ? dd[rr - lo] : 0u;
        const bool v = (u32)i16 < (d >> 24);
        const u32 gi = (d & 0xffffffu) + (v ? i16 : 0);
        rec[rr] = __builtin_nontemporal_load((const u32x3*)(R + (size_t)gi * 3));
        vmask |= v ? (1u << rr) : 0u;
      }
    };
    rounds(IC<0>{}, IC<8>{});
    if (nrounds > 8) rounds(IC<8>{}, IC<10>{});
    if (nrounds > 10) rounds(IC<10>{}, IC<12>{});
  };
  auto process = [&](auto fast_tag) AI {
    constexpr bool ALLOW_FAST = decltype(fast_tag)::value;
    u32 rk[MAXR];
    auto ranks = [&](auto lo_c, auto hi_c) AI {
      constexpr int lo = decltype(lo_c)::value, hi = decltype(hi_c)::value;
#pragma unroll
      for (int rr = lo; rr < hi; rr++) {
        const u32 g = (vmask >> rr) & 1u ? (rec[rr][2] >> 16) : 128u + (lane & 15);
        rk[rr] = atomicAdd(&hist[g], 1u);
      }
    };
#pragma unroll
    for (int rr = 8; rr < MAXR; rr++) rk[rr] = 0;
    ranks(IC<0>{}, IC<8>{});
    if (nrounds > 8) ranks(IC<8>{}, IC<10>{});
    if (nrounds > 10) ranks(IC<10>{}, IC<12>{});
    BAR();
    int n, glast;
    {
      u32x2 c = *(const u32x2*)(hist + 2 * lane);
      int s = c[0] + c[1];
      int inc = wave_incl_scan(s);
      u32x2 o; o[0] = inc - s; o[1] = o[0] + c[0];
      *(u32x2*)(off + 2 * lane) = o;
      n = __builtin_amdgcn_readlane(inc, 63);
      if (lane < 16) off[128 + lane] = n;
      const unsigned long long m1 = __ballot(c[1] != 0), m0 = __ballot(c[0] != 0);
      const int l1 = m1 ? 63 - __builtin_clzll(m1) : -1, l0 = m0 ? 63 - __builtin_clzll(m0) : -1;
      glast = max(2 * l1 + 1, 2 * l0); glast = glast < 0 ? 0 : glast;
    }
    const int ntiles = (n + 31) >> 5;
    auto scatter = [&](auto lo_c, auto hi_c) AI {
      constexpr int lo = decltype(lo_c)::value, hi = decltype(hi_c)::value;
      u32 ps[hi - lo];
#pragma unroll
      for (int rr = lo; rr < hi; rr++) {
        const bool v = (vmask >> rr) & 1u;
        const u32 g = v ? (rec[rr][2] >> 16) : 128u;
        ps[rr - lo] = off[g] + rk[rr];
      }
#pragma unroll
      for (int rr = lo; rr < hi; rr++) {
        const bool v = (vmask >> rr) & 1u;
        const u32 p = v ? ps[rr - lo] : (u32)(CAP + (lane & 31));
        u32x2 e; e[0] = rec[rr][0]; e[1] = rec[rr][1];
        *(u32x2*)(lds + L_EA + p * 8) = e;
        *(u32*)(lds + L_SG + (v ? p : p + (u32)SGPAD) * 4) = rec[rr][2];
      }
    };
    scatter(IC<0>{}, IC<8>{});
    if (nrounds > 8) scatter(IC<8>{}, IC<10>{});
    if (nrounds > 10) scatter(IC<10>{}, IC<12>{});
    {
      const int ps = n + lane + 64 * (wv & 3);
      if (wv >= 4 && ps < ntiles * 32 + SGPAD) {
        u32x2 z; z[0] = 0; z[1] = 0;
        if (ps < ntiles * 32) *(u32x2*)(lds + L_EA + ps * 8) = z;
        *(u32*)(lds + L_SG + ps * 4) = 127u << 16;
      }
    }
    BAR();

    int ln = lane;
    asm volatile("" : "+v"(ln));
    const int r = ln & 31, hh = ln >> 5;
    const int G4 = ln >> 4, i16 = ln & 15, tq = i16 >> 2, tp = i16 & 3, blk = G4 & 1;
    const int kk = ln >> 3, c8 = ln & 7;
    u32 gcst[4], aoff[4];
#pragma unroll
    for (int i = 0; i < 4; i++) gcst[i] = (u32)((c8 ^ i ^ (((kk >> 1) & 1) << 2)) * 16);
    {
      const int ii = r & 3, k2 = r >> 2;
#pragma unroll
      for (int s = 0; s < 4; s++)
        aoff[s] = lds0 + (u32)(L_GB + wv * 4096 + (ii * 8 + k2) * 128 + (((4 * hh + s) ^ ii ^ (((k2 >> 1) & 1) << 2)) * 16));
    }
    const int slot = (wv & 3) * 2 + (wv >> 2);
    auto cum = [&](int k) { return (ntiles * ((k >> 1) * 128 + (k & 1) * SPLIT)) >> 9; };
    const int t0 = cum(slot), t1 = cum(slot + 1);
    int head = 127;
    if (t0 < t1) {
      const u32 gb = lds0 + L_GB + wv * 4096;
      const u32 pb = lds0 + L_PB + wv * 1024;
      const u32 d2b = lds0 + L_D2 + wv * 512;
      const u32 a_vw = d2b + r * 8;
      const u32 a_vr = d2b + (4 * hh + tq) * 8;
      const u32 a_z = lds0 + L_ZERO;
      auto sk_read = [&](u32 a_k, u32x4& sk) AI {
        sk[0] = ldl<u32>(a_k); sk[1] = ldl<u32>(a_k + 4); sk[2] = ldl<u32>(a_k + 8); sk[3] = ldl<u32>(a_k + 12);
      };
      auto issue_h = [&](const u32x4& sk) AI {
        glds_idx(gb, sk[0], 128u, gcst[0], h);
        glds_idx(gb + 1024, sk[1], 128u, gcst[1], h);
        glds_idx(gb + 2048, sk[2], 128u, gcst[2], h);
        glds_idx(gb + 3072, sk[3], 128u, gcst[3], h);
      };
      u32 a_sk = lds0 + (u32)(L_SG + (t0 * 32 + 4 * kk) * 4), a_sr = lds0 + (u32)(L_SG + (t0 * 32 + r) * 4);
      { u32x4 sk; sk_read(a_sk, sk); issue_h(sk); a_sk += 128; }
      const int tl = (t0 + ln < t1) ? t0 + ln : t1 - 1;
      const int gfv = (int)(*(const u32*)(lds + L_SG + tl * 128) >> 16);
      const int glv = min((int)(*(const u32*)(lds + L_SG + tl * 128 + 124) >> 16), glast);
      const bool wide = __ballot(glv - gfv >= 8) != 0ull || (t1 - t0) > 64;
      int base = __builtin_amdgcn_readlane(gfv, 0);
      if ((int)__builtin_amdgcn_readfirstlane(off[base]) < t0 * 32) head = base;
      const int gspan = __builtin_amdgcn_readlane(glv, 63);
      f32x16 Y0 = {}, Y1 = {};
      u32 ap[2][2];
      auto load_ap = [&](int bs) AI {
#pragma unroll
        for (int ct = 0; ct < 2; ct++) {
          s16x4 v = trl(lds0 + (u32)(L_AP + (bs + 4 * hh + tq) * 128 + (32 * ct + 16 * blk + 4 * tp) * 2));
          u32x2 u = __builtin_bit_cast(u32x2, v);
          ap[ct][0] = u[0]; ap[ct][1] = u[1];
        }
      };
      u32 pa0, pa1;
      {
        const int b4 = (ln >> 4) & 1, b3 = (ln >> 3) & 1, b2 = (ln >> 2) & 1;
        const int j0 = 4 * b2 + 2 * b3 + b4, j1 = 8 + 2 * b3 + b4;
        pa0 = lds0 + (u32)(L_PACC + ((2 * (j0 / 3) + hh) * 4 + (j0 % 3)) * 4);
        pa1 = lds0 + (u32)(L_PACC + ((2 * (j1 / 3) + hh) * 4 + (j1 % 3)) * 4);
      }
      auto flush = [&](int bs) AI {
        u32 ma[4];
        float o0[4], o1[4];
        const u32 mrow = lds0 + (u32)(L_MACC + hh * (MS * 4) + 4 * r) + (u32)(bs * (MS * 4));
        if (bs + 7 <= gspan && (head == 127 || bs > head)) {
#pragma unroll
          for (int q = 0; q < 4; q++) ma[q] = mrow + (u32)(2 * q * (MS * 4));
        } else {
#pragma unroll
          for (int q = 0; q < 4; q++) {
            const int nd = bs + 2 * q + hh;
            u32 a = mrow + (u32)(2 * q * (MS * 4));
            a = nd > gspan ? lds0 + (u32)(L_MACC + MDUMMY * (MS * 4)) + 4 * r : a;
            a = nd == head ? lds0 + (u32)(L_HB + wv * (MS * 4)) + 4 * r : a;
            ma[q] = a;
          }
        }
#pragma unroll
        for (int q = 0; q < 4; q++) { o0[q] = ldl<float>(ma[q]); o1[q] = ldl<float>(ma[q] + 128); }
        float pr[12];
#pragma unroll
        for (int q = 0; q < 4; q++)
#pragma unroll
          for (int c = 1; c < 4; c++) pr[q * 3 + c - 1] = Y0[4 * q + c] * wx0 + Y1[4 * q + c] * wx1;
        SB();
#pragma unroll
        for (int q = 0; q < 4; q++) { stl<float>(ma[q], o0[q] + Y0[4 * q]); stl<float>(ma[q] + 128, o1[q] + Y1[4 * q]); }
        float x0, x1;
        reduce12(pr, x0, x1);
        masked_add2(pa0 + (u32)(bs * 16), x0, pa1 + (u32)(bs * 16), x1);
        const bf16x8 z8 = BF8((u32x4{0u, 0u, 0u, 0u}));
        Y0 = MFMA(z8, z8, f32x16{});
        Y1 = MFMA(z8, z8, f32x16{});
      };

      if (ALLOW_FAST && !wide) {
        int base1 = base, base2 = base;
        unsigned long long chg = 0;
        {
          int bb = base;
          for (int i = 1; i < t1 - t0; i++) {
            const int gl1 = __builtin_amdgcn_readlane(glv, i);
            if (gl1 >= bb + 8) { bb = __builtin_amdgcn_readlane(gfv, i); chg |= 1ull << i; }
          }
        }
        load_ap(base1);
        u32 bh = (u32)(base1 + 4 * hh);
        f32x16 XA0, XA1, XB0, XB1;
        const u32 a_ps = pb + ln * 16;
        const u32 a_d2w = d2b + ln * 8;
        const u32 st_row = lds0 + (u32)(L_ST + wv * 2048 + r * 64);
        const u32 a_st = lds0 + (u32)(L_ST + wv * 2048 + (4 * hh + tq) * 64 + (4 * blk + tp) * 8);
        u32 a_pd = lds0 + L_PD, a_lut = lds0 + L_LUT;
        PIN(a_pd); PIN(a_lut);
        u32 a_sp = lds0 + (u32)(L_SG + (t0 * 32 + ln) * 4);
        u32 a_x = hh ? d2b + r * 8 : lds0 + (u32)(L_EA + (t0 * 32 + r) * 8);
        const u32 incB = hh ? (u32)-256 : 256u;
        u32 st_prev = st_row, st_g;
        u32x2 vv;
        u32 sgp;
        auto geom = [&](u32 sg, const f32x4& ps, const f32x4& pd) AI {
          const float dx = pd[0] - ps[0], dy = pd[1] - ps[1], dz = pd[2] - ps[2];
          const bool pad = sg >= (127u << 16);
          u32 g0 = pk_bf16(1.0f, dx), g1 = pk_bf16(dy, dz); PIN(g0); PIN(g1);
          vv[0] = pad ? 0u : g0; vv[1] = pad ? 0u : g1;
          u32x2 d; d[0] = pk_bf16(dx * dx + dy * dy + dz * dz, 0.f); d[1] = d[0];
          stl<u32x2>(a_d2w, d);
          st_g = st_row + (sg >> 16) * 8;
        };
        auto st_write = [&](auto half_c, int bs) AI {
          constexpr int HALF = decltype(half_c)::value;
          u32 a = min(st_g - (u32)(bs * 8), st_row + 56u);
          half_write_b64<HALF>(st_prev, a, vv);
        };
        u32 sg_cur;
        {
          sgp = ldl<u32>(a_sp); a_sp += 256;
          glds_idx(pb, sgp, 16u, 0u, P4);
          const u32 sg0 = ldl<u32>(a_sr); a_sr += 128;
          const f32x4 pd = ldl<f32x4>(a_pd + __builtin_amdgcn_ubfe(sgp, 16, 7) * 16);
          const u32 sgn = ldl<u32>(a_sp); a_sp += 256;
          u32x4 sk; sk_read(a_sk, sk); a_sk += 128;
          sg_cur = ldl<u32>(a_sr); a_sr += 128;
          asm volatile("s_waitcnt vmcnt(0)" ::: "memory");
          bf16x8 A[4];
#pragma unroll
          for (int s = 0; s < 4; s++) A[s] = ldl<bf16x8>(aoff[s]);
          const f32x4 ps = ldl<f32x4>(a_ps);
          asm volatile("s_waitcnt lgkmcnt(0)" ::: "memory");
          issue_h(sk);
          geom(sgp, ps, pd);
          sgp = sgn;
          glds_idx(pb, sgp, 16u, 0u, P4);
          st_write(IC<0>{}, base1);
          const u32x2 xx = ldl<u32x2>(a_x); a_x += 256;
          const u32 t4 = min((sg0 >> 16) - bh, 4u);
          const u32x2 oh = ldl<u32x2>(a_lut + t4 * 8);
          XA0 = f32x16{}; XA1 = f32x16{};
#pragma unroll
          for (int s = 0; s < 4; s++) { XA0 = MFMA(A[s], Bw[s][0], XA0); XA1 = MFMA(A[s], Bw[s][1], XA1); }
          u32x4 a4, b0, b1;
          a4[0] = oh[0]; a4[1] = oh[1]; a4[2] = xx[0]; a4[3] = xx[1];
          b0[0] = ap[0][0]; b0[1] = ap[0][1]; b0[2] = xw[0][0]; b0[3] = xw[0][1];
          b1[0] = ap[1][0]; b1[1] = ap[1][1]; b1[2] = xw[1][0]; b1[3] = xw[1][1];
          XA0 = MFMA(BF8(a4), BF8(b0), XA0); XA1 = MFMA(BF8(a4), BF8(b1), XA1);
          asm volatile("s_nop 7\n\ts_nop 7" : "+v"(XA0), "+v"(XA1));
        }
        auto step = [&](auto next_tag, auto par_c, f32x16& C0, f32x16& C1, f32x16& N0, f32x16& N1, int t) AI {
          constexpr bool NEXT = decltype(next_tag)::value;
          constexpr int PAR = decltype(par_c)::value;
          if (NEXT) {
            if (__builtin_expect((chg >> (t + 1 - t0)) & 1ull, 0)) {
              base1 = __builtin_amdgcn_readlane(gfv, t + 1 - t0); load_ap(base1); bh = (u32)(base1 + 4 * hh);
            }
          }
          u32x4 M[2][2];
          bf16x8 A[4]; u32x4 sk; u32 sr, sgn; f32x4 ps, pd;
          u32x2 xx, oh;
          s16x4 sf[4];
          f32x2 tt[16];
          SB();
#define XP(p) (((p) >> 2) & 1 ? __builtin_shufflevector(C1, C1, 8 * ((p) >> 3) + 2 * ((p) & 3), 8 * ((p) >> 3) + 2 * ((p) & 3) + 1) \
                              : __builtin_shufflevector(C0, C0, 8 * ((p) >> 3) + 2 * ((p) & 3), 8 * ((p) >> 3) + 2 * ((p) & 3) + 1))
#define MSET(p, v) M[((p) >> 2) & 1][((p) >> 3) & 1][(p) & 3] = (v)
          static_for<0, 20>([&](auto ic) AI {
            constexpr int k = decltype(ic)::value - 1;
            constexpr bool R = (k - 1 >= 0 && k - 1 < 16), Cv = (k - 3 >= 0 && k - 3 < 16), E = (k + 1 < 16), Ad = (k >= 0 && k < 16), Mu = (k - 2 >= 0 && k - 2 < 16);
            if constexpr (R) {
              f32x2 v; v[0] = __builtin_amdgcn_rcpf(tt[(k - 1) & 15][0]); v[1] = __builtin_amdgcn_rcpf(tt[(k - 1) & 15][1]);
              tt[(k - 1) & 15] = v;
              SB();
            }
            if constexpr (Cv) {
              u32 mo;
              if constexpr (k == 18) asm volatile("v_cvt_pk_bf16_f32 %0, %1, %2\n\ts_nop 1" : "=v"(mo) : "v"(tt[(k - 3) & 15][0]), "v"(tt[(k - 3) & 15][1]));
              else asm volatile("v_cvt_pk_bf16_f32 %0, %1, %2" : "=v"(mo) : "v"(tt[(k - 3) & 15][0]), "v"(tt[(k - 3) & 15][1]));
              MSET((k - 3) & 15, mo);
              SB();
            }
            if constexpr (E) {
              const f32x2 xe = XP((k + 1) & 15);
              f32x2 v; v[0] = __builtin_amdgcn_exp2f(xe[0]); v[1] = __builtin_amdgcn_exp2f(xe[1]);
              tt[(k + 1) & 15] = v;
              SB();
            }
            if constexpr (Ad && Mu) {
              const f32x2 xm = XP((k - 2) & 15);
              asm volatile("v_pk_add_f32 %0, %0, 1.0 op_sel_hi:[1,0]\n\tv_pk_mul_f32 %1, %2, %1" : "+v"(tt[k & 15]), "+v"(tt[(k - 2) & 15]) : "v"(xm));
            } else {
              if constexpr (Ad) asm volatile("v_pk_add_f32 %0, %0, 1.0 op_sel_hi:[1,0]" : "+v"(tt[k & 15]));
              if constexpr (Mu) {
                const f32x2 xm = XP((k - 2) & 15);
                asm volatile("v_pk_mul_f32 %0, %1, %0" : "+v"(tt[(k - 2) & 15]) : "v"(xm));
              }
            }
            SB();
            if constexpr (k == -1) {
#pragma unroll
              for (int u = 0; u < 4; u++) sf[u] = trl(a_st + 512 * u);
              if constexpr (NEXT) {
                if constexpr (PAR == 1) asm volatile("s_waitcnt vmcnt(1)" ::: "memory"); else asm volatile("s_waitcnt vmcnt(0)" ::: "memory");
#pragma unroll
                for (int s = 0; s < 4; s++) A[s] = ldl<bf16x8>(aoff[s]);
                xx = ldl<u32x2>(a_x);
                oh = ldl<u32x2>(a_lut + min((sg_cur >> 16) - bh, 4u) * 8);
                sk_read(a_sk, sk);
                sr = ldl<u32>(a_sr);
              }
            }
            if constexpr (k == 1 && NEXT) {
              asm volatile("s_waitcnt lgkmcnt(0)" ::: "memory");
              issue_h(sk);
              stl<u32x2>(st_prev, u32x2{0u, 0u});
              st_write(IC<1 - PAR>{}, base1);
            }
            if constexpr (NEXT && k >= 2 && k < 12) {
              constexpr int m = k - 2, ks = m >> 1;
              if constexpr ((m & 1) == 0) {
                if constexpr (ks == 0) N0 = MFMA(A[0], Bw[0][0], f32x16{});
                else if constexpr (ks < 4) N0 = MFMA(A[ks & 3], Bw[ks & 3][0], N0);
                else { u32x4 a4, b0; a4[0] = oh[0]; a4[1] = oh[1]; a4[2] = xx[0]; a4[3] = xx[1];
                       b0[0] = ap[0][0]; b0[1] = ap[0][1]; b0[2] = xw[0][0]; b0[3] = xw[0][1]; N0 = MFMA(BF8(a4), BF8(b0), N0); }
              } else {
                if constexpr (ks == 0) N1 = MFMA(A[0], Bw[0][1], f32x16{});
                else if constexpr (ks < 4) N1 = MFMA(A[ks & 3], Bw[ks & 3][1], N1);
                else { u32x4 a4, b1; a4[0] = oh[0]; a4[1] = oh[1]; a4[2] = xx[0]; a4[3] = xx[1];
                       b1[0] = ap[1][0]; b1[1] = ap[1][1]; b1[2] = xw[1][0]; b1[3] = xw[1][1]; N1 = MFMA(BF8(a4), BF8(b1), N1); }
              }
            }
            if constexpr (k == 12) {
              Y0 = MFMA(mk8(sf[0], sf[1]), BF8(M[0][0]), Y0);
              if constexpr (NEXT && PAR == 0) {
                ps = ldl<f32x4>(a_ps);
                pd = ldl<f32x4>(a_pd + __builtin_amdgcn_ubfe(sgp, 16, 7) * 16);
                sgn = ldl<u32>(a_sp);
              }
            }
            if constexpr (k == 13) Y1 = MFMA(mk8(sf[0], sf[1]), BF8(M[1][0]), Y1);
            if constexpr (k == 14 && NEXT && PAR == 0) {
              geom(sgp, ps, pd);
              sgp = sgn; a_sp += 256; PIN(a_sp);
              asm volatile("s_waitcnt lgkmcnt(0)" ::: "memory");
              glds_idx(pb, sgp, 16u, 0u, P4);
            }
            if constexpr (k == 15 && NEXT) {
              sg_cur = sr; a_sk += 128; a_sr += 128; a_x += (PAR == 0 ? incB : 256u); PIN(a_sk); PIN(a_sr); PIN(a_x);
            }
            if constexpr (k == 16) Y0 = MFMA(mk8(sf[2], sf[3]), BF8(M[0][1]), Y0);
            SB();
          });
          Y1 = MFMA(mk8(sf[2], sf[3]), BF8(M[1][1]), Y1);
          SB();
          if (NEXT) { if (__builtin_expect(base1 != base2, 0)) { flush(base2); base2 = base1; } }
        };
        using Tt = std::integral_constant<bool, true>;
        using Ff = std::integral_constant<bool, false>;
        int t = t0;
        while (t + 2 < t1) { step(Tt{}, IC<0>{}, XA0, XA1, XB0, XB1, t); step(Tt{}, IC<1>{}, XB0, XB1, XA0, XA1, t + 1); t += 2; }
        if (t + 1 < t1) { step(Tt{}, IC<0>{}, XA0, XA1, XB0, XB1, t); step(Ff{}, IC<1>{}, XB0, XB1, XA0, XA1, t + 1); }
        else step(Ff{}, IC<0>{}, XA0, XA1, XB0, XB1, t);
        flush(base2);
        asm volatile("s_waitcnt vmcnt(0)" ::: "memory");
      } else {
        u32 slo, slen;
        auto load_sel = [&](int bs) AI {
          const int nl = bs + 4 * blk + tp;
          slo = off[nl]; slen = off[nl + 1] - slo;
        };
        const u32 a_ps = pb + r * 16;
        load_ap(base); load_sel(base);
        u32 sg_cur = ldl<u32>(a_sr); a_sr += 128;
        glds_idx_lo32(pb, sg_cur, P4);
        for (int t = t0; t < t1; t++) {
          const int Tb = t * 32;
          const u32x2 ea = *(const u32x2*)(lds + L_EA + (Tb + r) * 8);
          const f32x4 pd = *(const f32x4*)(lds + L_PD + ((sg_cur >> 16) & 127u) * 16);
          u32x4 sk; sk_read(a_sk, sk);
          const u32 sr = ldl<u32>(a_sr);
          asm volatile("s_waitcnt vmcnt(0)" ::: "memory");
          bf16x8 A[4];
#pragma unroll
          for (int s = 0; s < 4; s++) A[s] = ldl<bf16x8>(aoff[s]);
          const f32x4 ps = ldl<f32x4>(a_ps);
          asm volatile("s_waitcnt lgkmcnt(0)" ::: "memory");
          issue_h(sk);
          glds_idx_lo32(pb, sr, P4);
          const u32 sg = sg_cur;
          const float dx = pd[0] - ps[0], dy = pd[1] - ps[1], dz = pd[2] - ps[2];
          { u32x2 v; v[0] = pk_bf16(1.0f, dx); v[1] = pk_bf16(dy, dz); stl<u32x2>(a_vw, v); }
          const u32 d2 = pk_bf16(dx * dx + dy * dy + dz * dz, 0.f);
          sg_cur = sr; a_sk += 128; a_sr += 128;
          const int g = min((int)(sg >> 16), glast);
          const int gf = __builtin_amdgcn_readlane(g, 0), gl = __builtin_amdgcn_readlane(g, 31);
          if (gl >= base + 8) { flush(base); base = gf; load_ap(base); load_sel(base); }
          f32x16 X0 = {}, X1 = {};
#pragma unroll
          for (int s = 0; s < 4; s++) { X0 = MFMA(A[s], Bw[s][0], X0); X1 = MFMA(A[s], Bw[s][1], X1); }
          for (int wb = base;; wb += 8) {
            if (wb != base) load_ap(wb);
            const u32 t4 = (u32)g - (u32)(wb + 4 * hh);
            unsigned long long oh = 0x3F80ull << ((t4 & 3u) * 16u);
            if (t4 >= 4u) oh = 0ull;
            u32x4 a4; a4[0] = (u32)oh; a4[1] = (u32)(oh >> 32);
            a4[2] = wb == base ? (hh ? d2 : ea[0]) : 0u; a4[3] = wb == base ? (hh ? d2 : ea[1]) : 0u;
            u32x4 b0, b1;
            b0[0] = ap[0][0]; b0[1] = ap[0][1]; b0[2] = xw[0][0]; b0[3] = xw[0][1];
            b1[0] = ap[1][0]; b1[1] = ap[1][1]; b1[2] = xw[1][0]; b1[3] = xw[1][1];
            X0 = MFMA(BF8(a4), BF8(b0), X0);
            X1 = MFMA(BF8(a4), BF8(b1), X1);
            if (gl < wb + 8) { if (wb != base) load_ap(base); break; }
          }
          u32x4 M[2][2];
#pragma unroll
          for (int s = 0; s < 2; s++)
#pragma unroll
            for (int j = 0; j < 4; j++) {
              M[0][s][j] = silu2(X0[8 * s + 2 * j], X0[8 * s + 2 * j + 1]);
              M[1][s][j] = silu2(X1[8 * s + 2 * j], X1[8 * s + 2 * j + 1]);
            }
          for (;;) {
            s16x4 sf[4];
            const u32 d0 = (u32)(Tb + 4 * hh + tq) - slo;
#pragma unroll
            for (int u = 0; u < 4; u++) sf[u] = trl(((d0 + 8u * u) < slen ? a_vr : a_z) + 64 * u);
#pragma unroll
            for (int s = 0; s < 2; s++) {
              const bf16x8 S = mk8(sf[2 * s], sf[2 * s + 1]);
              Y0 = MFMA(S, BF8(M[0][s]), Y0);
              Y1 = MFMA(S, BF8(M[1][s]), Y1);
            }
            if (gl < base + 8) break;
            flush(base); base += 8; load_ap(base); load_sel(base);
          }
        }
        flush(base);
        asm volatile("s_waitcnt vmcnt(0)" ::: "memory");
      }
    }
    if (ALLOW_FAST) {
      glds16(lds0 + L_GB + wv * 4096, (u32)(wv * 2048 + lane * 16), wh);
      glds16(lds0 + L_GB + wv * 4096 + 1024, (u32)(wv * 2048 + 1024 + lane * 16), wh);
    }
    if (lane == 0) misc[16 + slot] = (u32)head;
    BAR();
    {
      float* hb = (float*)(lds + L_HB) + wv * MS;
      const int hprev = slot > 0 ? (int)misc[16 + slot - 1] : 127, hnext = slot < 7 ? (int)misc[16 + slot + 1] : 127;
      const bool uniq = (hprev != head) && (hnext != head);
      if (head != 127) {
        const float v = hb[lane];
        hb[lane] = 0.f;
        if (uniq) macc[head * MS + lane] += v;
        else atomicAdd(&macc[head * MS + lane], v);
      }
      if (tid < 144) hist[tid] = 0;
    }
  };

  load_records(0, 4 * MAXR);
  int Pmax = 0, ntot = 0;
  {
    for (int i = tid; i < (99 + 8) * MS / 4; i += 512) *(f32x4*)(macc + 4 * i) = f32x4{0.f, 0.f, 0.f, 0.f};
    if (tid < 105) *(f32x4*)(pacc + 4 * tid) = f32x4{0.f, 0.f, 0.f, 0.f};
    if (tid < 64) *(u32*)(lds + L_ZERO + tid * 4) = 0;
    if (tid < 5) { u32x2 v; v[0] = tid == 0 ? 0x3F80u : (tid == 1 ? 0x3F800000u : 0u); v[1] = tid == 2 ? 0x3F80u : (tid == 3 ? 0x3F800000u : 0u); *(u32x2*)(lds + L_LUT + tid * 8) = v; }
#pragma unroll
    for (int i = 0; i < 2; i++) *(u32x4*)(lds + L_ST + (tid + 512 * i) * 16) = u32x4{0, 0, 0, 0};
    if (tid < 128) { f32x4 v; v[0] = bf2f(pd0); v[1] = bf2f(pd1); v[2] = bf2f(pd2); v[3] = 0.f; *(f32x4*)(lds + L_PD + tid * 16) = v; }
    if (tid < 144) hist[tid] = 0;
    if (lane == 0) { misc[wv] = (u32)P; misc[8 + wv] = (u32)csum; }
    u32x4 o;
#pragma unroll
    for (int i = 0; i < 4; i++) o[i] = pk_bf16(bflo(wB[i]) * NLOG2E, bfhi(wB[i]) * NLOG2E);
    const u32x4 z4 = {0, 0, 0, 0};
    *(u32x4*)(lds + L_WE + tid * 16) = wA;
    *(u32x4*)(lds + L_WE + 8192 + tid * 16) = o;
    if (tid < 40) *(u32x4*)(lds + L_WE + 16384 + tid * 16) = wC;
    *(u32x4*)(lds + L_H2 + tid * 16) = tid < nn * 8 ? hA : z4;
    if (tid < 288) *(u32x4*)(lds + L_H2 + 8192 + tid * 16) = tid + 512 < nn * 8 ? hB : z4;
    BAR();
#pragma unroll
    for (int i = 0; i < 8; i++) { Pmax = max(Pmax, (int)misc[i]); ntot += (int)misc[8 + i]; }
    Pmax = __builtin_amdgcn_readfirstlane(Pmax); ntot = __builtin_amdgcn_readfirstlane(ntot);
#pragma unroll
    for (int s = 0; s < 4; s++)
#pragma unroll
      for (int ct = 0; ct < 2; ct++) {
        const u32 p = lds0 + (u32)(L_WE + (64 + 32 * hh + 8 * s + tq) * 128 + (32 * ct + 16 * blk + 4 * tp) * 2);
        Bw[s][ct] = mk8(trl(p), trl(p + 4 * 128));
      }
#pragma unroll
    for (int ct = 0; ct < 2; ct++) {
      const u16* wt = (const u16*)(lds + L_WE + 16384);
      const int col = 32 * ct + r;
      const float w0 = bf2f(wt[col]) * NLOG2E, w1 = bf2f(wt[64 + col]) * NLOG2E, w2 = bf2f(wt[128 + col]) * NLOG2E,
                  w3 = bf2f(wt[192 + col]) * NLOG2E, w4 = bf2f(wt[256 + col]) * NLOG2E;
      const u32 hi = pk_bf16(w0, 0.f) & 0xffffu;
      const u32 lo = pk_bf16(w0 - bf2f(hi), 0.f) & 0xffffu;
      xw[ct][0] = hh ? hi : pk_bf16(w1, w2);
      xw[ct][1] = hh ? lo : pk_bf16(w3, w4);
    }
    {
      const int nt = wv >> 1, ct = wv & 1;
      f32x16 acc = {};
#pragma unroll
      for (int s = 0; s < 4; s++) {
        const u32 p = lds0 + (u32)(L_WE + (32 * hh + 8 * s + tq) * 128 + (32 * ct + 16 * blk + 4 * tp) * 2);
        bf16x8 aw = mk8(trl(p), trl(p + 4 * 128));
        bf16x8 bh = *(const bf16x8*)(lds + L_H2 + min(32 * nt + r, 99) * 128 + (4 * hh + s) * 16);
        acc = MFMA(aw, bh, acc);
      }
      const int node = 32 * nt + r;
      char* ap = lds + (node < 105 ? L_AP + node * 128 : L_DESC + 2048 + wv * 128) + (32 * ct + 4 * hh) * 2;
#pragma unroll
      for (int q = 0; q < 4; q++) {
        u32x2 v;
        v[0] = pk_bf16(acc[4 * q] * NLOG2E, acc[4 * q + 1] * NLOG2E);
        v[1] = pk_bf16(acc[4 * q + 2] * NLOG2E, acc[4 * q + 3] * NLOG2E);
        *(u32x2*)(ap + 16 * q) = v;
      }
    }
  }
  if (Pmax <= 4 * MAXR && ntot <= CAP) {
    process(std::integral_constant<bool, true>{});
  } else {
    for (int plo = 0; plo < Pmax; plo += FPPB) {
      BAR();
      load_records(plo, FPPB);
      process(std::integral_constant<bool, false>{});
    }
  }

  if (!(Pmax <= 4 * MAXR && ntot <= CAP)) {
    glds16(lds0 + L_GB + wv * 4096, (u32)(wv * 2048 + lane * 16), wh);
    glds16(lds0 + L_GB + wv * 4096 + 1024, (u32)(wv * 2048 + 1024 + lane * 16), wh);
  }
  asm volatile("s_waitcnt vmcnt(0)" ::: "memory");
  BAR();
  {
    int ln = lane;
    asm volatile("" : "+v"(ln));
    const int r = ln & 31, hh = ln >> 5;
    const int G4 = ln >> 4, i16 = ln & 15, tq = i16 >> 2, tp = i16 & 3, blk = G4 & 1;
    const int nt = wv >> 1, ct = wv & 1;
    const int node = 32 * nt + r;
    const int nrow = node < NB ? node : MDUMMY;
    f32x16 acc = {};
#pragma unroll
    for (int s = 0; s < 8; s++) {
      const int kb = (s < 4) ? (32 * hh + 8 * s) : (64 + 32 * hh + 8 * (s - 4));
      const int k0 = kb + tq;
      const u32 p = lds0 + (u32)(L_GB + (k0 >> 4) * 4096 + (k0 & 15) * 128 + (32 * ct + 16 * blk + 4 * tp) * 2);
      const bf16x8 aw = mk8(trl(p), trl(p + 4 * 128));
      bf16x8 bb;
      if (s < 4) {
        bb = *(const bf16x8*)(lds + L_H2 + min(node, 99) * 128 + (4 * hh + s) * 16);
      } else {
        const float* mp = macc + nrow * MS + 32 * hh + 8 * (s - 4);
        const f32x4 m0 = *(const f32x4*)mp, m1 = *(const f32x4*)(mp + 4);
        u32x4 pk;
        pk[0] = pk_bf16(m0[0] * NLN2, m0[1] * NLN2); pk[1] = pk_bf16(m0[2] * NLN2, m0[3] * NLN2);
        pk[2] = pk_bf16(m1[0] * NLN2, m1[1] * NLN2); pk[3] = pk_bf16(m1[2] * NLN2, m1[3] * NLN2);
        bb = __builtin_bit_cast(bf16x8, pk);
      }
      acc = MFMA(aw, bb, acc);
    }
#pragma unroll
    for (int q = 0; q < 4; q++) {
      float u[4];
#pragma unroll
      for (int i = 0; i < 4; i++) {
        const float x = bf2f(pk_bf16(acc[4 * q + i], 0.f) & 0xffffu);
        u[i] = x * __builtin_amdgcn_rcpf(1.0f + __builtin_amdgcn_exp2f(x * NLOG2E));
      }
      u32x2 v; v[0] = pk_bf16(u[0], u[1]); v[1] = pk_bf16(u[2], u[3]);
      *(u32x2*)(lds + L_UT + node * 144 + (32 * ct + 8 * q + 4 * hh) * 2) = v;
    }
  }
  BAR();
#pragma unroll
  for (int k = 0; k < 2; k++) {
    const int i = tid + 512 * k;
    if (i < nn * 8) {
      const int row = i >> 3, chn = i & 7;
      const u32x4 u = *(const u32x4*)(lds + L_UT + row * 144 + chn * 16);
      const u32x4 hv = *(const u32x4*)(lds + L_H2 + i * 16);
      u32x4 o;
#pragma unroll
      for (int j = 0; j < 4; j++) o[j] = pk_bf16(bflo(hv[j]) + bflo(u[j]), bfhi(hv[j]) + bfhi(u[j]));
      *(u32x4*)(out + (size_t)(node0 + row) * 64 + chn * 8) = o;
    }
  }
  if (tid < nn * 3) {
    const int nd = tid / 3, c = tid - nd * 3;
    const float pv = *(const float*)(lds + L_PD + nd * 16 + c * 4);
    const float dlt = bf2f(pk_bf16(pacc[nd * 4 + c], 0.f) & 0xffffu);
    out[(size_t)N * 64 + (size_t)node0 * 3 + tid] = (u16)(pk_bf16(pv + dlt, 0.f) & 0xffffu);
  }
}

extern "C" void kernel_launch(void* const* d_in, const int* in_sizes, int n_in, void* d_out, int out_size, void* d_ws,
                              size_t ws_size, hipStream_t stream) {
  const u16* h = (const u16*)d_in[0];
  const u16* pos = (const u16*)d_in[1];
  const u16* ea = (const u16*)d_in[2];
  const int* src = (const int*)d_in[3];
  const int* dst = (const int*)d_in[4];
  const u16* we = (const u16*)d_in[5];
  const u16* wx = (const u16*)d_in[6];
  const u16* wh = (const u16*)d_in[7];
  const int N = in_sizes[0] / 64, E = in_sizes[3];
  char* ws = (char*)d_ws;
  u32* R = (u32*)ws;
  f32x4* P4 = (f32x4*)(ws + (size_t)10 * 1048576);
  u32* T = (u32*)(ws + (size_t)11 * 1048576);
  (void)hipFuncSetAttribute((const void*)k1_partition, hipFuncAttributeMaxDynamicSharedMemorySize, K1_LDS);
  (void)hipFuncSetAttribute((const void*)k2_main, hipFuncAttributeMaxDynamicSharedMemorySize, K2_LDS);
  k1_partition<<<(E + CH - 1) / CH, 1024, K1_LDS, stream>>>(pos, (const u32x4*)ea, (const i32x4*)src, (const i32x4*)dst, R, P4, T, N, E);
  k2_main<<<(N + NB - 1) / NB, 512, K2_LDS, stream>>>(h, pos, we, wx, wh, R, P4, T, (u16*)d_out, N);
}
```
